```python
import torch

def _softmax(x, axis=-1):
    return torch.softmax(x, dim=axis)
def _gelu(x):
    return torch.nn.functional.gelu(x, approximate="tanh")
def _silu(x):
    return torch.nn.functional.silu(x)
def _sigmoid(x):
    return torch.sigmoid(x)
def _layernorm(x, axis=-1, eps=1e-5):
    m = x.mean(dim=axis, keepdim=True)
    v = x.var(dim=axis, keepdim=True, unbiased=False)
    return (x - m) / torch.sqrt(v + eps)
def _scatter_add(tgt, idx, src, axis=0):
    return tgt.clone().index_add_(axis, idx, src)
def _segment_sum(src, seg, n):
    out = torch.zeros((n,) + src.shape[1:], dtype=src.dtype)
    return out.index_add_(0, seg, src)
def _pad(x, pw):
    flat = tuple(v for pair in reversed(pw) for v in pair)
    return torch.nn.functional.pad(x, flat)

kernel_name = 'recurrent_scan__sliding_block_cached__swiglu_ffn__decode'

def setup_inputs():

    _w = (lambda a: (lambda t: t.to(torch.bfloat16) if t.is_floating_point() else t)(torch.as_tensor(a)))
    def _A_setup():
        B, Sq, D = 1, 1, 576
        Hq, Hkv, Sk, Dh = 32, 16, 65536, 128
        assert Hq % Hkv == 0, (Hq, Hkv)
        return {
            "x": _w((lambda *s: torch.randn(*s, dtype=torch.bfloat16) * 0.05)(B, Sq, D)),
            "k_cache": _w((lambda *s: torch.randn(*s, dtype=torch.bfloat16) * 0.8)(B, Hkv, Sk, Dh)),
            "v_cache": _w((lambda *s: torch.randn(*s, dtype=torch.bfloat16) * 0.2)(B, Hkv, Sk, Dh)),
            "wq": _w((lambda *s: torch.randn(*s, dtype=torch.bfloat16) * 0.8)(D, Hq * Dh) * (D ** -0.5)),
            "wo": _w((lambda *s: torch.randn(*s, dtype=torch.bfloat16) * 0.2)(Hq * Dh, D) * ((Hq * Dh) ** -0.5)),
        }
    _A_in = _A_setup()

    def _B_setup():
        B, S, D, F = 1, 1, 576, 4608
        return {
            "x": _w((lambda *s: torch.randn(*s, dtype=torch.bfloat16) * 0.05)(B, S, D)),
            "w_gate": _w((lambda *s: torch.randn(*s, dtype=torch.bfloat16) * 0.1)(D, F)),
            "w_up": _w((lambda *s: torch.randn(*s, dtype=torch.bfloat16) * 0.1)(D, F)),
            "w_down": _w((lambda *s: torch.randn(*s, dtype=torch.bfloat16) * 0.1)(F, D)),
        }
    _B_in = _B_setup()

    return {
        "A_x": _A_in["x"],
        "A_k_cache": _A_in["k_cache"],
        "A_v_cache": _A_in["v_cache"],
        "A_wq": _A_in["wq"],
        "A_wo": _A_in["wo"],
        "B_x": _B_in["x"],
        "B_w_gate": _B_in["w_gate"],
        "B_w_up": _B_in["w_up"],
        "B_w_down": _B_in["w_down"],
    }

def reference(A_x, A_k_cache, A_v_cache, A_wq, A_wo, B_x, B_w_gate, B_w_up, B_w_down):
    def _A_ref(x, k_cache, v_cache, wq, wo):
        B, Sq, D = x.shape
        Hq, Dh = 32, 128
        Hkv, Sk = k_cache.shape[1], k_cache.shape[2]
        W = min(4096, max(256, Sk // 4))
        rep = Hq // Hkv
        q = (lambda x, *d: x.permute(*d))((x @ wq).reshape(B, Sq, Hq, Dh), 0, 2, 1, 3)
        k = (lambda x, n, axis: torch.repeat_interleave(x, n, dim=axis))(k_cache[:, :, Sk - W:, :], rep, axis=1)
        v = (lambda x, n, axis: torch.repeat_interleave(x, n, dim=axis))(v_cache[:, :, Sk - W:, :], rep, axis=1)
        scores = torch.einsum("bhqd,bhkd->bhqk", q, k) * (Dh ** -0.5)
        p = _softmax(scores, axis=-1)
        o = torch.einsum("bhqk,bhkd->bhqd", p, v)
        return (lambda x, *d: x.permute(*d))(o, 0, 2, 1, 3).reshape(B, Sq, Hq * Dh) @ wo

    def _B_ref(x, w_gate, w_up, w_down):
        g = _silu(x @ w_gate)
        u = x @ w_up
        return (g * u) @ w_down

    x = A_x
    xn = _layernorm(x, axis=-1)
    s = _A_ref(xn, A_k_cache, A_v_cache, A_wq, A_wo)
    g = _silu(_B_ref(xn, B_w_gate, B_w_up, B_w_down))
    return x + s * g
```

```cpp
#include <hip/hip_runtime.h>
#include <hip/hip_bf16.h>
#include <stdint.h>

typedef unsigned short bf16_t;
typedef unsigned v4u __attribute__((ext_vector_type(4)));

constexpr int D = 576, HQ = 32, HKV = 16, SK = 65536, DH = 128, W = 4096, F = 4608;
constexpr int NQ = HQ * DH;

__device__ __forceinline__ float bf2f(unsigned short b) { return __uint_as_float(((unsigned)b) << 16); }
__device__ __forceinline__ unsigned short f2bf(float f) {
  unsigned u = __float_as_uint(f);
  u += 0x7FFFu + ((u >> 16) & 1u);
  return (unsigned short)(u >> 16);
}
__device__ __forceinline__ float rbf(float f) { return bf2f(f2bf(f)); }

__device__ void layernorm_lds(const bf16_t* x, float* xn, float* red) {
  int tid = threadIdx.x;
  if (tid < 64) {
    float v[9]; float s = 0.f;
    for (int i = 0; i < 9; ++i) { v[i] = bf2f(x[tid + 64 * i]); s += v[i]; }
    for (int m = 32; m >= 1; m >>= 1) s += __shfl_xor(s, m);
    float mean = s / 576.f;
    float q = 0.f;
    for (int i = 0; i < 9; ++i) { float d = v[i] - mean; q += d * d; }
    for (int m = 32; m >= 1; m >>= 1) q += __shfl_xor(q, m);
    float var = rbf(q / 576.f);
    float mb = rbf(mean);
    float den = rbf(sqrtf(rbf(var + 1e-5f)));
    for (int i = 0; i < 9; ++i) xn[tid + 64 * i] = rbf(rbf(v[i] - mb) / den);
  }
  __syncthreads();
}

__global__ void __launch_bounds__(256) k1(const bf16_t* x, const bf16_t* wq, const bf16_t* wg, const bf16_t* wu, float* q, float* g, float* u) {
  __shared__ float xn[D]; __shared__ float red[256 * 8];
  layernorm_lds(x, xn, red);
  int b = blockIdx.x; const bf16_t* Wm; float* out; int N; int c0;
  if (b < 64) { Wm = wq; out = q; N = NQ; c0 = b * 64; }
  else if (b < 64 + 72) { Wm = wg; out = g; N = F; c0 = (b - 64) * 64; }
  else { Wm = wu; out = u; N = F; c0 = (b - 136) * 64; }
  int tid = threadIdx.x; int cc = tid & 7, r0 = tid >> 3;
  float acc[8] = {0, 0, 0, 0, 0, 0, 0, 0};
  for (int r = r0; r < D; r += 32) {
    v4u w = *(const v4u*)(Wm + (size_t)r * N + c0 + cc * 8);
    float xv = xn[r];
    for (int k = 0; k < 4; ++k) { acc[2 * k] += xv * __uint_as_float(w[k] << 16); acc[2 * k + 1] += xv * __uint_as_float(w[k] & 0xffff0000u); }
  }
  for (int k = 0; k < 8; ++k) red[tid * 8 + k] = acc[k];
  __syncthreads();
  if (tid < 64) { float s = 0.f; int c = tid >> 3, k = tid & 7; for (int r = 0; r < 32; ++r) s += red[(r * 8 + c) * 8 + k]; out[c0 + tid] = rbf(s); }
}

__global__ void __launch_bounds__(256) k2(const float* q, const bf16_t* kc, const bf16_t* vc, float* pm, float* pl, float* po) {
  __shared__ float sc[2][256]; __shared__ float red[16][2][128]; __shared__ float mm[2], ll[2];
  int s = blockIdx.x, h = blockIdx.y; int tid = threadIdx.x; int c = tid & 15, r = tid >> 4;
  const bf16_t* kb = kc + ((size_t)h * SK + (SK - W) + s * 256) * DH; const bf16_t* vb = vc + ((size_t)h * SK + (SK - W) + s * 256) * DH;
  float q0[8], q1[8];
  for (int k = 0; k < 8; ++k) { q0[k] = q[(2 * h) * DH + c * 8 + k]; q1[k] = q[(2 * h + 1) * DH + c * 8 + k]; }
  for (int it = 0; it < 16; ++it) {
    int key = it * 16 + r; v4u w = *(const v4u*)(kb + (size_t)key * DH + c * 8);
    float a0 = 0.f, a1 = 0.f;
    for (int k = 0; k < 4; ++k) { float lo = __uint_as_float(w[k] << 16), hi = __uint_as_float(w[k] & 0xffff0000u); a0 += lo * q0[2 * k] + hi * q0[2 * k + 1]; a1 += lo * q1[2 * k] + hi * q1[2 * k + 1]; }
    for (int m = 8; m >= 1; m >>= 1) { a0 += __shfl_xor(a0, m); a1 += __shfl_xor(a1, m); }
    if (c == 0) { sc[0][key] = a0 * 0.08838834764831845f; sc[1][key] = a1 * 0.08838834764831845f; }
  }
  __syncthreads();
  if (tid < 128) {
    int hh = tid >> 6, l = tid & 63; float m = -INFINITY;
    for (int i = 0; i < 4; ++i) m = fmaxf(m, sc[hh][l + 64 * i]);
    for (int o = 32; o >= 1; o >>= 1) m = fmaxf(m, __shfl_xor(m, o));
    float sum = 0.f;
    for (int i = 0; i < 4; ++i) { float p = __expf(sc[hh][l + 64 * i] - m); sc[hh][l + 64 * i] = p; sum += p; }
    for (int o = 32; o >= 1; o >>= 1) sum += __shfl_xor(sum, o);
    if (l == 0) { mm[hh] = m; ll[hh] = sum; }
  }
  __syncthreads();
  float a0[8] = {0, 0, 0, 0, 0, 0, 0, 0}, a1[8] = {0, 0, 0, 0, 0, 0, 0, 0};
  for (int it = 0; it < 16; ++it) {
    int key = it * 16 + r; v4u w = *(const v4u*)(vb + (size_t)key * DH + c * 8);
    float p0 = sc[0][key], p1 = sc[1][key];
    for (int k = 0; k < 4; ++k) { float lo = __uint_as_float(w[k] << 16), hi = __uint_as_float(w[k] & 0xffff0000u); a0[2 * k] += p0 * lo; a0[2 * k + 1] += p0 * hi; a1[2 * k] += p1 * lo; a1[2 * k + 1] += p1 * hi; }
  }
  for (int k = 0; k < 8; ++k) { red[r][0][c * 8 + k] = a0[k]; red[r][1][c * 8 + k] = a1[k]; }
  __syncthreads();
  { int hh = tid >> 7, d = tid & 127; float sum = 0.f; for (int i = 0; i < 16; ++i) sum += red[i][hh][d];
    int head = 2 * h + hh; po[((size_t)head * 16 + s) * DH + d] = sum;
    if (d == 0) { pm[head * 16 + s] = mm[hh]; pl[head * 16 + s] = ll[hh]; } }
}

__global__ void __launch_bounds__(576) k3(const float* pm, const float* pl, const float* po, const bf16_t* wo, const float* g, const float* u, const bf16_t* wd, float* sp, float* fp) {
  __shared__ float vec[128]; __shared__ float red[8][576];
  int b = blockIdx.x, tid = threadIdx.x; const bf16_t* Wm; float* out;
  if (b < 32) {
    if (tid < 128) {
      float M = -INFINITY; for (int s = 0; s < 16; ++s) M = fmaxf(M, pm[b * 16 + s]);
      float L = 0.f, o = 0.f; for (int s = 0; s < 16; ++s) { float w = __expf(pm[b * 16 + s] - M); L += w * pl[b * 16 + s]; o += w * po[((size_t)b * 16 + s) * DH + tid]; }
      vec[tid] = rbf(o / L);
    }
    Wm = wo + (size_t)b * 128 * D; out = sp + b * D;
  } else {
    int bb = b - 32;
    if (tid < 128) { float gv = g[bb * 128 + tid], uv = u[bb * 128 + tid]; float sl = rbf(gv / (1.f + __expf(-gv))); vec[tid] = rbf(sl * uv); }
    Wm = wd + (size_t)bb * 128 * D; out = fp + bb * D;
  }
  __syncthreads();
  int cc = tid % 72, r0 = tid / 72; float acc[8] = {0, 0, 0, 0, 0, 0, 0, 0};
  for (int r = r0; r < 128; r += 8) {
    v4u w = *(const v4u*)(Wm + (size_t)r * D + cc * 8); float xv = vec[r];
    for (int k = 0; k < 4; ++k) { acc[2 * k] += xv * __uint_as_float(w[k] << 16); acc[2 * k + 1] += xv * __uint_as_float(w[k] & 0xffff0000u); }
  }
  for (int k = 0; k < 8; ++k) red[r0][cc * 8 + k] = acc[k];
  __syncthreads();
  float s = 0.f; for (int r = 0; r < 8; ++r) s += red[r][tid];
  out[tid] = s;
}

__global__ void __launch_bounds__(576) k4(const bf16_t* x, const float* sp, const float* fp, bf16_t* out) {
  int tid = threadIdx.x; float s = 0.f, f = 0.f;
  for (int i = 0; i < 32; ++i) s += sp[i * D + tid];
  for (int i = 0; i < 36; ++i) f += fp[i * D + tid];
  s = rbf(s); f = rbf(f); float g = rbf(f / (1.f + __expf(-f)));
  out[tid] = f2bf(bf2f(x[tid]) + rbf(s * g));
}

extern "C" void kernel_launch(void* const* d_in, const int* in_sizes, int n_in, void* d_out, int out_size, void* d_ws, size_t ws_size, hipStream_t stream) {
  const bf16_t* x = (const bf16_t*)d_in[0]; const bf16_t* kc = (const bf16_t*)d_in[1]; const bf16_t* vc = (const bf16_t*)d_in[2];
  const bf16_t* wq = (const bf16_t*)d_in[3]; const bf16_t* wo = (const bf16_t*)d_in[4];
  const bf16_t* wg = (const bf16_t*)d_in[6]; const bf16_t* wu = (const bf16_t*)d_in[7]; const bf16_t* wd = (const bf16_t*)d_in[8];
  float* ws = (float*)d_ws;
  float* q = ws; float* g = q + 4096; float* u = g + 4608; float* pm = u + 4608; float* pl = pm + 512; float* po = pl + 512; float* sp = po + 32 * 16 * 128; float* fp = sp + 32 * 576;
  k1<<<208, 256, 0, stream>>>(x, wq, wg, wu, q, g, u);
  k2<<<dim3(16, 16), 256, 0, stream>>>(q, kc, vc, pm, pl, po);
  k3<<<68, 576, 0, stream>>>(pm, pl, po, wo, g, u, wd, sp, fp);
  k4<<<1, 576, 0, stream>>>(x, sp, fp, (bf16_t*)d_out);
}
```

```cpp
#include <hip/hip_runtime.h>
#include <stdint.h>

typedef unsigned short bf16_t;
typedef unsigned u32;
typedef unsigned long long u64;
typedef u32 v4u __attribute__((ext_vector_type(4)));
typedef u32 v2u __attribute__((ext_vector_type(2)));
typedef float f32x4 __attribute__((ext_vector_type(4)));
typedef short s16x4 __attribute__((ext_vector_type(4)));
typedef short s16x8 __attribute__((ext_vector_type(8)));
typedef __bf16 bf16x2 __attribute__((ext_vector_type(2)));
typedef __bf16 bf16x8 __attribute__((ext_vector_type(8)));
#define LAS __attribute__((address_space(3)))
#define GAS __attribute__((address_space(1)))
typedef __amdgpu_buffer_rsrc_t rsrc_t;

namespace {

constexpr int D = 576, SK = 65536, DH = 128, WIN = 4096, F = 4608, NQ = 4096;
constexpr int NWAVES = 12, NTHR = NWAVES * 64;

constexpr u32 WS_EPOCH = 0;
constexpr u32 WS_Q = 4096;
constexpr u32 WS_GM = WS_Q + 256 * 256;
constexpr u32 WS_GP = WS_GM + 64 * 8 * 256;
constexpr u32 WS_OP = WS_GP + 256 * 256;
constexpr u32 WS_ML = WS_OP + 32 * 16 * 512;
constexpr u32 WS_FIN = WS_ML + 32 * 16 * 16;
constexpr u32 WS_END = WS_FIN + 72 * 2 * 64 * 32;
constexpr u32 WS_STAMP = 4u << 20;

constexpr u32 L_K = 0;
constexpr u32 L_V = 65536;
constexpr u32 L_WO = 131072;
constexpr u32 L_XN = L_WO + 18432;
constexpr u32 L_QI = L_XN + 2304;
constexpr u32 L_PI = L_QI + 512;
constexpr u32 L_OW = L_PI + 1024;
constexpr u32 L_HI = L_OW + 4096;
constexpr u32 L_OI = L_HI + 160;
constexpr u32 L_WT = L_OI + 128;
constexpr u32 L_MX = L_WT + 64;
constexpr u32 L_TAG = L_MX + 64;
constexpr u32 L_FLG = L_TAG + 32;
constexpr u32 L_END = L_FLG + 128;
static_assert(L_END <= 163840, "LDS");
enum { FL_Q, FL_VISS, FL_MX, FL_OW, FL_OPUB, FL_H, FL_O, FL_WOL, FL_LS, FL_FIN, FL_G0, FL_TAG, FL_W0, FL_M2, FL_QPUB, FL_HPUB, FL_DL, FL_VH, FL_DH };

constexpr int AUX_NT = 2, AUX_SC1 = 16;
constexpr u32 SPIN_CAP = 1u << 22;
constexpr u32 OOB = 0x7FFFFFF0u;
#define KVROT 1
#define KVTILE(t, s) ((t) * 16 + (((s) + KVROT * (t)) & 15))
#define Q_DELAY 4
#define H_DELAY 8
#define ML_DELAY 12
#define O_DELAY 8
#define F_DELAY 10
#define DH_LEFT 10
#define VH_AT 10

extern __shared__ __attribute__((aligned(16))) unsigned char smem[];

__device__ __forceinline__ float lo2f(u32 p) { return __uint_as_float(p << 16); }
__device__ __forceinline__ float hi2f(u32 p) { return __uint_as_float(p & 0xFFFF0000u); }
__device__ __forceinline__ u32 pk(float a, float b) {
  bf16x2 r = {(__bf16)a, (__bf16)b};
  return __builtin_bit_cast(u32, r);
}
__device__ __forceinline__ float rbf(float f) { return lo2f(pk(f, f)); }
__device__ __forceinline__ float sel(bool c, float a, float b) { return c ? a : b; }
__device__ __forceinline__ float sel4(u32 g, float a, float b, float c, float d) {
  const bool b0 = g & 1, b1 = g & 2;
  const float lo = b0 ? b : a, hi = b0 ? d : c;
  return b1 ? hi : lo;
}
__device__ __forceinline__ rsrc_t mk(const void* p, u32 bytes) {
  return __builtin_amdgcn_make_buffer_rsrc((void*)p, 0, bytes, 0x00020000);
}
__device__ __forceinline__ float dot2(u32 w, u32 x, float acc) {
  return __builtin_amdgcn_fdot2_f32_bf16(__builtin_bit_cast(bf16x2, w), __builtin_bit_cast(bf16x2, x), acc, false);
}
template <int CTRL> __device__ __forceinline__ float dpp(float x) {
  return __builtin_bit_cast(float, __builtin_amdgcn_mov_dpp(__builtin_bit_cast(int, x), CTRL, 0xf, 0xf, true));
}
constexpr int DPP_XOR1 = 0xB1, DPP_XOR2 = 0x4E, DPP_HMIRROR = 0x141, DPP_MIRROR = 0x140, DPP_ROR4 = 0x124, DPP_ROR8 = 0x128;
__device__ __forceinline__ float swap16_add(float a, float b) {
  auto s = __builtin_amdgcn_permlane16_swap(__float_as_uint(a), __float_as_uint(b), false, false);
  return __uint_as_float(s[0]) + __uint_as_float(s[1]);
}
__device__ __forceinline__ float swap32_add(float a, float b) {
  auto s = __builtin_amdgcn_permlane32_swap(__float_as_uint(a), __float_as_uint(b), false, false);
  return __uint_as_float(s[0]) + __uint_as_float(s[1]);
}
__device__ __forceinline__ float swap16_max(float a) {
  auto s = __builtin_amdgcn_permlane16_swap(__float_as_uint(a), __float_as_uint(a), false, false);
  return fmaxf(__uint_as_float(s[0]), __uint_as_float(s[1]));
}
__device__ __forceinline__ float swap32_max(float a) {
  auto s = __builtin_amdgcn_permlane32_swap(__float_as_uint(a), __float_as_uint(a), false, false);
  return fmaxf(__uint_as_float(s[0]), __uint_as_float(s[1]));
}
__device__ __forceinline__ float wave_sum(float x) {
  x += dpp<DPP_XOR1>(x); x += dpp<DPP_XOR2>(x); x += dpp<DPP_HMIRROR>(x); x += dpp<DPP_MIRROR>(x);
  x = swap16_add(x, x); x = swap32_add(x, x);
  return x;
}
__device__ __forceinline__ float wave_max(float x) {
  x = fmaxf(x, dpp<DPP_XOR1>(x)); x = fmaxf(x, dpp<DPP_XOR2>(x)); x = fmaxf(x, dpp<DPP_HMIRROR>(x)); x = fmaxf(x, dpp<DPP_MIRROR>(x));
  x = swap16_max(x); x = swap32_max(x);
  return x;
}

struct Tag { u32 t0, t1, t2, t3; };
__device__ __forceinline__ u32 q22(float v, u32 t) { return ((__float_as_uint(v) + 0x200u) & 0xFFFFFC00u) | t; }
__device__ __forceinline__ float r22(float v) { return __uint_as_float((__float_as_uint(v) + 0x200u) & 0xFFFFFC00u); }
__device__ __forceinline__ v4u quad(float a, float b, float c, float d, Tag t) { return v4u{q22(a, t.t0), q22(b, t.t1), q22(c, t.t2), q22(d, t.t3)}; }
__device__ __forceinline__ float gv(u32 d) { return __uint_as_float(d & 0xFFFFFC00u); }
__device__ __forceinline__ f32x4 gv4(v4u v) { return f32x4{gv(v.x), gv(v.y), gv(v.z), gv(v.w)}; }
struct Chk {
  u32 a = 0;
  __device__ __forceinline__ void add(v4u v, Tag t) {
    a = __builtin_amdgcn_bitop3_b32(v.x, t.t0, a, 0xBE); a = __builtin_amdgcn_bitop3_b32(v.y, t.t1, a, 0xBE);
    a = __builtin_amdgcn_bitop3_b32(v.z, t.t2, a, 0xBE); a = __builtin_amdgcn_bitop3_b32(v.w, t.t3, a, 0xBE);
  }
  __device__ __forceinline__ bool ok() const { return __all((a & 0x3FFu) == 0u); }
};
__device__ __forceinline__ void st_quad(rsrc_t rs, u32 off, v4u q) { __builtin_amdgcn_raw_buffer_store_b128(q, rs, off, 0, AUX_SC1); }
__device__ __forceinline__ v4u ld_quad(rsrc_t rs, u32 off) { return __builtin_amdgcn_raw_buffer_load_b128(rs, off, 0, AUX_SC1); }
#define GATHER(g, N, OFFEXPR)                                              \
  _Pragma("unroll 1") for (u32 sp_ = 0; sp_ < SPIN_CAP; ++sp_) {          \
    asm volatile("" ::: "memory");                                         \
    Chk c_;                                                                \
    _Pragma("unroll") for (int i = 0; i < N; ++i) g[i] = ld_quad(rws, OFFEXPR); \
    _Pragma("unroll") for (int i = 0; i < N; ++i) c_.add(g[i], tag);      \
    COUNT_ATTEMPT();                                                       \
    if (c_.ok()) break;                                                    \
  }

__device__ __forceinline__ volatile LAS u32* flg(int i) { return (volatile LAS u32*)(smem + L_FLG) + i; }
__device__ __forceinline__ void wait_flag(int i, u32 want) {
  volatile LAS u32* p = flg(i);
#pragma unroll 1
  for (u32 s = 0; s < SPIN_CAP; ++s) {
    if (__builtin_amdgcn_readfirstlane(*p) >= want) break;
    __builtin_amdgcn_s_sleep(1);
  }
  asm volatile("" ::: "memory");
}
__device__ __forceinline__ void wait_flag_tight(int i, u32 want) {
  volatile LAS u32* p = flg(i);
#pragma unroll 1
  for (u32 s = 0; s < SPIN_CAP; ++s) {
    if (__builtin_amdgcn_readfirstlane(*p) >= want) break;
  }
  asm volatile("" ::: "memory");
}
__device__ __forceinline__ Tag gettag() {
  wait_flag(FL_TAG, 1);
  const v4u tg = *(const LAS v4u*)(smem + L_TAG);
  Tag t{(u32)__builtin_amdgcn_readfirstlane(tg.x), (u32)__builtin_amdgcn_readfirstlane(tg.y), (u32)__builtin_amdgcn_readfirstlane(tg.z), (u32)__builtin_amdgcn_readfirstlane(tg.w)};
  asm volatile("" : "+s"(t.t0), "+s"(t.t1), "+s"(t.t2), "+s"(t.t3));
  return t;
}
__device__ __forceinline__ void set_flag(int i) {
  asm volatile("s_waitcnt lgkmcnt(0)" ::: "memory");
  *flg(i) = 1u;
  asm volatile("" ::: "memory");
}
__device__ __forceinline__ void add_flag(int i) {
  asm volatile("s_waitcnt lgkmcnt(0)" ::: "memory");
  if ((threadIdx.x & 63) == 0) __hip_atomic_fetch_add((LAS u32*)(smem + L_FLG) + i, 1u, __ATOMIC_RELAXED, __HIP_MEMORY_SCOPE_WORKGROUP);
  asm volatile("" ::: "memory");
}
__device__ __forceinline__ u32 bump_flag(int i) {
  asm volatile("s_waitcnt lgkmcnt(0)" ::: "memory");
  u32 r = 0;
  if ((threadIdx.x & 63) == 0) r = __hip_atomic_fetch_add((LAS u32*)(smem + L_FLG) + i, 1u, __ATOMIC_RELAXED, __HIP_MEMORY_SCOPE_WORKGROUP);
  r = __builtin_amdgcn_readfirstlane(r);
  asm volatile("" ::: "memory");
  return r;
}
__device__ __forceinline__ void dma16(rsrc_t rs, u32 voff, u32 soff, u32 ldsoff) {
  asm volatile("s_mov_b32 m0, %0\n\ts_nop 0\n\tbuffer_load_dwordx4 %1, %2, %3 offen nt lds" ::"s"(ldsoff), "v"(voff), "s"(rs), "s"(soff) : "m0", "memory");
}
#define COUNT_ATTEMPT() do { } while (0)
#define VMCNT_(n) asm volatile("s_waitcnt vmcnt(" #n ")" ::: "memory")
#define VMCNT(n) VMCNT_(n)
#define SB() __builtin_amdgcn_sched_barrier(0)
#define BARRIER() do { __builtin_amdgcn_sched_barrier(0); asm volatile("s_waitcnt lgkmcnt(0)" ::: "memory"); __builtin_amdgcn_s_barrier(); asm volatile("" ::: "memory"); __builtin_amdgcn_sched_barrier(0); } while (0)

#define STAMP(i) do { } while (0)
#define STAMP_FLUSH() do { } while (0)

struct Args {
  const bf16_t* x; const bf16_t* kc; const bf16_t* vc; const bf16_t* wq; const bf16_t* wo;
  const bf16_t* wg; const bf16_t* wu; const bf16_t* wd; bf16_t* out; unsigned char* ws;
};

__device__ __forceinline__ float silu_b(float v) {
  return rbf(v * __builtin_amdgcn_rcpf(1.f + __builtin_amdgcn_exp2f(-1.4426950408889634f * v)));
}
__device__ __forceinline__ v4u quad_of_lanes(float v, Tag tag) {
  return quad(dpp<0x00>(v), dpp<0x55>(v), dpp<0xAA>(v), dpp<0xFF>(v), tag);
}

__device__ __forceinline__ void down_gemv(int part, int j, int lane, Tag tag, rsrc_t rws) {
  const int r = j >> 2, c4 = j & 3;
  const u32 lr = lane >> 4, q4 = (lane & 15) >> 2, p4 = lane & 3;
  const u32 badr = L_K + (8 * lr + q4) * 288 + p4 * 8 + part * 96;
  const u32 row4 = 64 + 4 * lr + q4;
  const u32 badr4 = L_K + (row4 < 72 ? row4 : 71) * 288 + p4 * 8 + part * 96;
  const u32 cq = c4 * 36 + (3 * part + lr) * 4 + q4;
  u32 off = (lr < 3 && p4 == 0) ? WS_FIN + (((cq >> 1) * 2 + 1) * 64 + r) * 32 + (cq & 1) * 16 : OOB;
  asm volatile("" : "+v"(off));
  wait_flag(FL_DL, 1);
  s16x4 b[3][5];
#pragma unroll
  for (int t = 0; t < 3; ++t) {
#pragma unroll
    for (int u = 0; u < 4; ++u) b[t][u] = __builtin_amdgcn_ds_read_tr16_b64_v4i16((LAS s16x4*)(smem + badr + ((u >> 1) * 32 + (u & 1) * 4) * 288 + t * 32));
    b[t][4] = __builtin_amdgcn_ds_read_tr16_b64_v4i16((LAS s16x4*)(smem + badr4 + t * 32));
  }
#pragma unroll
  for (int t = 0; t < 3; ++t)
#pragma unroll
    for (int u = 0; u < 5; ++u) asm volatile("" : "+v"(b[t][u]));
  wait_flag_tight(FL_H, 1);
  const s16x8 a0 = *(const LAS s16x8*)(smem + L_HI + lr * 16), a1 = *(const LAS s16x8*)(smem + L_HI + 64 + lr * 16);
  const s16x4 a2 = *(const LAS s16x4*)(smem + L_HI + 128 + lr * 8);
  f32x4 acc[3];
#pragma unroll
  for (int t = 0; t < 3; ++t) {
    f32x4 c = {0.f, 0.f, 0.f, 0.f};
    c = __builtin_amdgcn_mfma_f32_16x16x16bf16_1k(a2, b[t][4], c, 0, 0, 0);
    c = __builtin_amdgcn_mfma_f32_16x16x32_bf16(__builtin_bit_cast(bf16x8, a0), __builtin_bit_cast(bf16x8, __builtin_shufflevector(b[t][0], b[t][1], 0, 1, 2, 3, 4, 5, 6, 7)), c, 0, 0, 0);
    c = __builtin_amdgcn_mfma_f32_16x16x32_bf16(__builtin_bit_cast(bf16x8, a1), __builtin_bit_cast(bf16x8, __builtin_shufflevector(b[t][2], b[t][3], 0, 1, 2, 3, 4, 5, 6, 7)), c, 0, 0, 0);
    acc[t] = c;
  }
  st_quad(rws, off, quad_of_lanes(sel4(lr, acc[0][0], acc[1][0], acc[2][0], acc[2][0]), tag));
  add_flag(FL_FIN);
}

__global__ void __launch_bounds__(NTHR, 3) fused_kernel(Args a) {
  const int tid = threadIdx.x, lane = tid & 63;
  const int wave = __builtin_amdgcn_readfirstlane(tid >> 6);
  const int b = blockIdx.x;
  const int j = (b & 7) * 32 + (b >> 3);
  STAMP(0);
  if (wave == 11) {
    if (lane < 32) *flg(lane) = 0u;
    if (lane >= 32 && lane < 36) *(LAS u32*)(smem + L_HI + 144 + (lane - 32) * 4) = 0u;
  }
  BARRIER();

  if (__builtin_expect(wave < 4, 1)) {
    __builtin_amdgcn_s_setprio(3);
    const int kp = j & 3, r = j >> 2;
    const bf16_t* wm; u32 row0, stride, strip, nrows, pub;
    if (wave == 0) { wm = a.wq; row0 = 144 * kp; stride = NQ * 2; strip = r; nrows = 144; pub = WS_Q + j * 256; }
    else if (wave < 3) {
      const int quarter = 2 * (kp & 1) + (wave - 1);
      wm = (kp >> 1) ? a.wu : a.wg; row0 = 144 * quarter; stride = F * 2; nrows = 144;
      strip = (r & ~7) | ((r + 3 * wave) & 7);
      pub = WS_GM + ((strip * 2 + (kp >> 1)) * 4 + quarter) * 256;
    } else { wm = ((j >> 4) & 1) ? a.wu : a.wg; row0 = 36 * (j & 15); stride = F * 2; strip = 64 + (j >> 5); nrows = 36; pub = WS_GP + j * 256; }
    const rsrc_t rw = mk((const unsigned char*)wm + (size_t)row0 * stride + strip * 128, (nrows - 1) * stride + 128);
    const rsrc_t rx = mk(a.x, D * 2);
    const rsrc_t rws = mk(a.ws, WS_END);
    const u64 ep = __hip_atomic_load((GAS u64*)(a.ws + WS_EPOCH), __ATOMIC_RELAXED, __HIP_MEMORY_SCOPE_AGENT);
    u32 xr[5];
#pragma unroll
    for (int i = 0; i < 5; ++i) xr[i] = __builtin_amdgcn_raw_buffer_load_b32(rx, lane * 4, i * 256, 0);
    const u32 rs8 = lane >> 3;
    const u32 voff = rs8 * stride + (lane & 7) * 16;
    u64 tg = ep;
    Tag tag;
    auto layernorm = [&](v4u (&av)[18]) {
#pragma unroll
      for (int i = 0; i < 5; ++i) asm volatile("" : "+v"(xr[i]));
      asm volatile("" : "+v"(tg));
      tg += 1;
      {
        const u64 T = ((u64)(u32)__builtin_amdgcn_readfirstlane((u32)(tg >> 32)) << 32) | (u32)__builtin_amdgcn_readfirstlane((u32)tg);
        const u64 H = T * 0x9E3779B97F4A7C15ull;
        tag = Tag{(u32)T & 0x3FFu, (u32)(H >> 54), (u32)(H >> 44) & 0x3FFu, (u32)(H >> 34) & 0x3FFu};
        if (wave == 0) {
          if (lane == 0) { *(LAS v4u*)(smem + L_TAG) = v4u{tag.t0, tag.t1, tag.t2, tag.t3}; *(LAS v2u*)(smem + L_TAG + 16) = v2u{(u32)T, (u32)(T >> 32)}; }
          set_flag(FL_TAG);
        }
      }
      float s = 0.f, q = 0.f;
#pragma unroll
      for (int i = 0; i < 5; ++i) { s = dot2(xr[i], 0x3F803F80u, s); q = dot2(xr[i], xr[i], q); }
      s = wave_sum(s); q = wave_sum(q);
      const float mean = s * (1.f / 576.f);
      const float var = rbf(fmaxf(fmaf(-mean, mean, q * (1.f / 576.f)), 0.f));
      const float mb = rbf(mean);
      const float inv = __builtin_amdgcn_rcpf(rbf(__builtin_amdgcn_sqrtf(rbf(var + 1e-5f))));
      LAS v2u* xn = (LAS v2u*)(smem + L_XN);
#pragma unroll
      for (int i = 0; i < 5; ++i) {
        const u32 d = pk(lo2f(xr[i]) - mb, hi2f(xr[i]) - mb);
        const u32 n = pk(lo2f(d) * inv, hi2f(d) * inv);
        if (i < 4 || lane < 32) xn[i * 64 + lane] = v2u{__builtin_amdgcn_perm(n, n, 0x01000100u), __builtin_amdgcn_perm(n, n, 0x03020302u)};
      }
      asm volatile("s_waitcnt lgkmcnt(0)" ::: "memory");
      u32 xp[18];
      const LAS u32* xs = (const LAS u32*)(smem + L_XN) + row0 + rs8;
#pragma unroll
      for (int i = 0; i < 18; ++i) xp[i] = xs[(wave == 3 && i > 4) ? 0 : 8 * i];
      if (wave == 3 && rs8 >= 4) xp[4] = 0u;
      u32 msk[4];
#pragma unroll
      for (int d = 0; d < 4; ++d) msk[d] = (((lane & 7) >> 1) == d) ? ((lane & 1) ? 0xFFFF0000u : 0x0000FFFFu) : 0u;
#pragma unroll
      for (int i = 0; i < 18; ++i) av[i] = v4u{xp[i] & msk[0], xp[i] & msk[1], xp[i] & msk[2], xp[i] & msk[3]};
    };
    auto gemv_publish = [&](const v4u (&av)[18], const v4u (&w)[18]) {
      f32x4 da = {0.f, 0.f, 0.f, 0.f}, db = {0.f, 0.f, 0.f, 0.f};
#pragma unroll
      for (int i = 0; i < 18; ++i) {
        if (i & 1) db = __builtin_amdgcn_mfma_f32_16x16x32_bf16(__builtin_bit_cast(bf16x8, av[i]), __builtin_bit_cast(bf16x8, w[i]), db, 0, 0, 0);
        else da = __builtin_amdgcn_mfma_f32_16x16x32_bf16(__builtin_bit_cast(bf16x8, av[i]), __builtin_bit_cast(bf16x8, w[i]), da, 0, 0, 0);
      }
      da += db;
      f32x4 res;
#pragma unroll
      for (int k = 0; k < 4; ++k) {
        const float up = dpp<DPP_ROR8>(da[k]);
        res[k] = da[k] + __uint_as_float(__builtin_amdgcn_permlane32_swap(__float_as_uint(up), __float_as_uint(up), false, false)[1]);
      }
      const u32 off = ((lane & 0x28) == 0) ? pub + (2 * (lane & 7) + (lane >> 4)) * 16 : OOB;
      st_quad(rws, off, quad(res[0], res[1], res[2], res[3], tag));
    };
    if (__builtin_expect(wave == 0, 1)) {
      v4u w[18], av[18];
#pragma unroll
      for (int i = 0; i < 18; ++i) w[i] = __builtin_amdgcn_raw_buffer_load_b128(rw, voff, 8 * i * stride, AUX_NT);
      SB();
      set_flag(FL_W0);
      STAMP(1);
      layernorm(av);
      STAMP(2);
      gemv_publish(av, w);
      set_flag(FL_QPUB);
      STAMP(3);
      {
        const u32 bm = WS_GM + r * 2048 + lane * 16;
        const u32 bp = WS_GP + ((r >> 3) * 32 + (lane >> 1)) * 256 + ((r & 7) * 2 + (lane & 1)) * 16;
        wait_flag(FL_HPUB, 3);
        __builtin_amdgcn_s_sleep(H_DELAY);
        wait_flag(FL_DH, 1);
        STAMP(4);
        v4u g[3];
        GATHER(g, 3, (i < 2 ? bm + i * 1024 : bp));
        STAMP(5);
        const f32x4 gg = gv4(g[0]), uu = gv4(g[1]), tt = gv4(g[2]);
        const bool tail = lane >= 16;
        float hv[4];
#pragma unroll
        for (int k = 0; k < 4; ++k) {
          float m = swap16_add(gg[k], uu[k]);
          m = swap32_add(m, m);
          float t = tt[k];
          t += dpp<DPP_XOR2>(t); t += dpp<DPP_ROR4>(t); t += dpp<DPP_ROR8>(t); t = swap16_add(t, t);
          const auto sm = __builtin_amdgcn_permlane16_swap(__float_as_uint(m), __float_as_uint(m), false, false);
          const auto stl = __builtin_amdgcn_permlane32_swap(__float_as_uint(t), __float_as_uint(t), false, false);
          const float gs = __uint_as_float(tail ? stl[0] : sm[0]), us = __uint_as_float(tail ? stl[1] : sm[1]);
          hv[k] = silu_b(rbf(gs)) * rbf(us);
        }
        if (lane < 18) *(LAS v2u*)(smem + L_HI + lane * 8) = v2u{pk(hv[0], hv[1]), pk(hv[2], hv[3])};
        set_flag(FL_H);
        STAMP(6);
      }
      if ((j & 31) < 9) {
        const int f = (j >> 5) * 9 + (j & 31);
        const u32 mycol = f * 8 + 4 * (lane & 1) + 2 * ((lane >> 4) & 1) + ((lane >> 3) & 1);
        const u32 xo = __builtin_amdgcn_raw_buffer_load_b16(rx, mycol * 2, 0, 0);
        const u32 base = WS_FIN + f * 4096 + lane * 16;
        wait_flag(FL_FIN, 5);
        __builtin_amdgcn_s_sleep(F_DELAY);
        STAMP(7);
        v4u g[4];
        GATHER(g, 4, base + i * 1024);
        STAMP(1);
        const f32x4 sv = gv4(g[0]) + gv4(g[1]), fv = gv4(g[2]) + gv4(g[3]);
        float a4[4];
#pragma unroll
        for (int k = 0; k < 4; ++k) a4[k] = swap32_add(sv[k], fv[k]);
        const float a0 = swap16_add(a4[0], a4[2]), a1 = swap16_add(a4[1], a4[3]);
        float b0 = a0 + dpp<DPP_XOR2>(a0), b1 = a1 + dpp<DPP_XOR2>(a1);
        b0 += dpp<DPP_ROR4>(b0); b1 += dpp<DPP_ROR4>(b1);
        b0 += dpp<DPP_ROR8>(b0); b1 += dpp<DPP_ROR8>(b1);
        const float t = (lane & 8) ? b1 : b0;
        const auto sf = __builtin_amdgcn_permlane32_swap(__float_as_uint(t), __float_as_uint(t), false, false);
        const float sb = rbf(__uint_as_float(sf[0])), fb = rbf(__uint_as_float(sf[1]));
        const float o = lo2f(xo) + rbf(sb * silu_b(fb));
        if ((lane & 0x26) == 0) *(GAS bf16_t*)((GAS unsigned char*)a.out + mycol * 2) = (bf16_t)(pk(o, o) & 0xFFFFu);
        if (f == 0 && lane == 0) { const v2u nx = *(const LAS v2u*)(smem + L_TAG + 16); __hip_atomic_store((GAS u64*)(a.ws + WS_EPOCH), (u64)nx.x | ((u64)nx.y << 32), __ATOMIC_RELAXED, __HIP_MEMORY_SCOPE_AGENT); }
        STAMP(2);
      }
    } else {
      v4u w[18], av[18];
      layernorm(av);
      STAMP(2);
      __builtin_amdgcn_s_setprio(1);
#pragma unroll
      for (int i = 0; i < 18; ++i) asm volatile("" : "+v"(av[i]));
      wait_flag(FL_VISS, 4);
#pragma unroll
      for (int i = 0; i < 18; ++i) w[i] = __builtin_amdgcn_raw_buffer_load_b128(rw, voff, 8 * i * stride, AUX_NT);
      SB();
      add_flag(FL_G0);
      __builtin_amdgcn_s_setprio(3);
      STAMP(5);
      gemv_publish(av, w);
      add_flag(FL_HPUB);
      STAMP(3);
      if (wave >= 2) {
        down_gemv(wave - 1, j, lane, tag, rws);
        STAMP(6);
      }
    }
  } else if (__builtin_expect(wave < 8, 1)) {
    __builtin_amdgcn_s_setprio(0);
    const int w = wave - 4, g = j >> 4, s = j & 15;
    const rsrc_t rk = mk(a.kc + ((size_t)g * SK + (SK - WIN)) * DH, WIN * DH * 2);
    const rsrc_t rv = mk(a.vc + ((size_t)g * SK + (SK - WIN)) * DH, WIN * DH * 2);
    const rsrc_t rws = mk(a.ws, WS_END);
    const u32 lr = lane >> 4;
    u32 vo[4];
#pragma unroll
    for (int sub = 0; sub < 4; ++sub) { vo[sub] = sub * 1024 + lr * 256 + ((((lane & 15) ^ (lr << 2)) ^ sub) << 4); asm volatile("" : "+v"(vo[sub])); }
    wait_flag(FL_W0, 1);
#pragma unroll
    for (int p = 0; p < 16; ++p) dma16(rk, vo[p & 3], KVTILE(4 * w + (p >> 2), s) * 4096, L_K + w * 16384 + p * 1024);
#pragma unroll
    for (int p = 0; p < 16; ++p) {
      dma16(rv, vo[p & 3], KVTILE(4 * w + (p >> 2), s) * 4096, L_V + w * 16384 + p * 1024);
      if (p == VH_AT && w == 0) *flg(FL_VH) = 1u;
    }
    add_flag(FL_VISS);
    __builtin_amdgcn_s_setprio(3);
    STAMP(1);
    const Tag tag = gettag();
    const u32 krow = lane & 15;
    u32 kadr = L_K + w * 16384 + krow * 256 + ((lr ^ (((krow & 3) << 2) | ((krow >> 2) & 3))) << 4);
    const u32 q4 = (lane & 15) >> 2, p4 = lane & 3;
    u32 va[2][8];
#pragma unroll
    for (int t = 0; t < 2; ++t) {
      const u32 row = 8 * (lr & 1) + 4 * t + q4;
      const u32 base = L_V + w * 16384 + (lr >> 1) * 4096 + row * 256 + (((p4 >> 1) ^ ((q4 << 2) | (2 * (lr & 1) + t))) << 4) + 8 * (p4 & 1);
#pragma unroll
      for (int c = 0; c < 8; ++c) { va[t][c] = base ^ (c << 5); asm volatile("" : "+v"(va[t][c])); }
    }
    u32 ob = WS_OP + (((2 * g + (lane >> 5)) * 16 + s) * 32 + (lane & 31)) * 16;
    asm volatile("" : "+v"(kadr), "+v"(ob));
    VMCNT(16);
    s16x8 kf[16];
#pragma unroll
    for (int i = 0; i < 4; ++i)
#pragma unroll
      for (int st = 0; st < 4; ++st) kf[i * 4 + st] = *(const LAS s16x8*)(smem + ((kadr ^ (st << 6)) + i * 4096));
#pragma unroll
    for (int i = 0; i < 16; ++i) asm volatile("" : "+v"(kf[i]));
    add_flag(FL_MX);
    STAMP(2);
    wait_flag(FL_Q, 1);
    STAMP(3);
    s16x8 qf[4];
#pragma unroll
    for (int st = 0; st < 4; ++st) qf[st] = *(const LAS s16x8*)(smem + L_QI + (lane & 1) * 256 + st * 64 + lr * 16);
    f32x4 sc[4];
#pragma unroll
    for (int i = 0; i < 4; ++i) sc[i] = f32x4{0.f, 0.f, 0.f, 0.f};
#pragma unroll
    for (int st = 0; st < 4; ++st)
#pragma unroll
      for (int i = 0; i < 4; ++i)
        sc[i] = __builtin_amdgcn_mfma_f32_16x16x32_bf16(__builtin_bit_cast(bf16x8, qf[st]), __builtin_bit_cast(bf16x8, kf[i * 4 + st]), sc[i], 0, 0, 0);
    constexpr float C2 = 0.08838834764831845f * 1.4426950408889634f;
    const float s0 = sel4(lr, sc[0][0], sc[1][0], sc[2][0], sc[3][0]) * C2;
    const float s1 = sel4(lr, sc[0][1], sc[1][1], sc[2][1], sc[3][1]) * C2;
    LAS float* mx = (LAS float*)(smem + L_MX);
    {
      const float wm0 = wave_max(s0), wm1 = wave_max(s1);
      if (lane == 0) *(LAS v2u*)(mx + 2 * w) = v2u{__float_as_uint(wm0), __float_as_uint(wm1)};
    }
    add_flag(FL_M2);
    wait_flag_tight(FL_M2, 4);
    const f32x4 ma = *(const LAS f32x4*)(mx), mb4 = *(const LAS f32x4*)(mx + 4);
    const float m0 = r22(fmaxf(fmaxf(ma[0], ma[2]), fmaxf(mb4[0], mb4[2])));
    const float m1 = r22(fmaxf(fmaxf(ma[1], ma[3]), fmaxf(mb4[1], mb4[3])));
    const u32 pp = pk(__builtin_amdgcn_exp2f(s0 - m0), __builtin_amdgcn_exp2f(s1 - m1));
    LAS bf16_t* pi = (LAS bf16_t*)(smem + L_PI + w * 256);
    pi[lane] = (bf16_t)(pp & 0xFFFFu); pi[64 + lane] = (bf16_t)(pp >> 16);
    {
      const float l0 = wave_sum(lo2f(pp)), l1 = wave_sum(hi2f(pp));
      if (lane == 0) *(LAS v2u*)(mx + 8 + 2 * w) = v2u{__float_as_uint(l0), __float_as_uint(l1)};
    }
    s16x8 pf[2];
#pragma unroll
    for (int ks = 0; ks < 2; ++ks) pf[ks] = *(const LAS s16x8*)(smem + L_PI + w * 256 + (lane & 1) * 128 + ks * 64 + lr * 16);
    f32x4 oc[8];
#pragma unroll
    for (int c = 0; c < 8; ++c) oc[c] = f32x4{0.f, 0.f, 0.f, 0.f};
    STAMP(4);
#pragma unroll
    for (int ks = 0; ks < 2; ++ks) {
      if (ks == 0) VMCNT(8); else VMCNT(0);
      if (ks == 1) STAMP(5);
      s16x4 b0[8], b1[8];
#pragma unroll
      for (int c = 0; c < 8; ++c) {
        b0[c] = __builtin_amdgcn_ds_read_tr16_b64_v4i16((LAS s16x4*)(smem + va[0][c] + ks * 8192));
        b1[c] = __builtin_amdgcn_ds_read_tr16_b64_v4i16((LAS s16x4*)(smem + va[1][c] + ks * 8192));
      }
      SB();
#pragma unroll
      for (int c = 0; c < 8; ++c) {
        const s16x8 bb = __builtin_shufflevector(b0[c], b1[c], 0, 1, 2, 3, 4, 5, 6, 7);
        oc[c] = __builtin_amdgcn_mfma_f32_16x16x32_bf16(__builtin_bit_cast(bf16x8, pf[ks]), __builtin_bit_cast(bf16x8, bb), oc[c], 0, 0, 0);
      }
      SB();
    }
    if (lane < 16) {
      LAS float* ow = (LAS float*)(smem + L_OW) + w * 256 + lane;
#pragma unroll
      for (int c = 0; c < 8; ++c) { ow[c * 16] = oc[c][0]; ow[128 + c * 16] = oc[c][1]; }
    }
    const u32 arrived = bump_flag(FL_OW);
    STAMP(6);
    if (arrived == 3) {
      f32x4 t[4];
#pragma unroll
      for (int u = 0; u < 4; ++u) t[u] = *(const LAS f32x4*)(smem + L_OW + u * 1024 + lane * 16);
      const f32x4 la = *(const LAS f32x4*)(mx + 8), lb = *(const LAS f32x4*)(mx + 12);
      const f32x4 o = (t[0] + t[1]) + (t[2] + t[3]);
      st_quad(rws, ob, quad(o[0], o[1], o[2], o[3], tag));
      const float L = (lane >> 5) ? (la[1] + la[3]) + (lb[1] + lb[3]) : (la[0] + la[2]) + (lb[0] + lb[2]);
      st_quad(rws, (lane & 31) == 0 ? WS_ML + ((2 * g + (lane >> 5)) * 16 + s) * 16 : OOB, quad((lane >> 5) ? m1 : m0, L, 0.f, 0.f, tag));
      set_flag(FL_OPUB);
      STAMP(7);
    }
  } else if (wave < 10) {
    __builtin_amdgcn_s_setprio(1);
    const int w2 = wave & 1;
    const int r = j >> 2, c4 = j & 3;
    const rsrc_t rw = mk(a.wo, NQ * D * 2);
    const rsrc_t rws = mk(a.ws, WS_END);
    u32 vo[9];
#pragma unroll
    for (int i = 0; i < 9; ++i) {
      const u32 id = (w2 + 2 * i) * 64 + lane;
      const u32 row = (id * 3641u) >> 16, c = id - row * 18;
      vo[i] = (64 * r + row) * 1152 + c4 * 288 + c * 16;
      asm volatile("" : "+v"(vo[i]));
    }
    const Tag tag = gettag();
    wait_flag(FL_G0, 3);
    STAMP(1);
#pragma unroll
    for (int i = 0; i < 9; ++i) dma16(rw, vo[i], 0, L_WO + (w2 + 2 * i) * 1024);
    __builtin_amdgcn_s_setprio(3);
    STAMP(2);
    VMCNT(0);
    add_flag(FL_WOL);
    wait_flag(FL_WOL, 2);
    STAMP(3);
    const u32 lr = lane >> 4, q4 = (lane & 15) >> 2, p4 = lane & 3;
    const u32 badr = L_WO + (8 * lr + q4) * 288 + p4 * 8 + w2 * 32;
    s16x4 bb[5][4];
#pragma unroll
    for (int t = 0; t < 5; ++t)
#pragma unroll
      for (int u = 0; u < 4; ++u)
        bb[t][u] = __builtin_amdgcn_ds_read_tr16_b64_v4i16((LAS s16x4*)(smem + badr + ((u >> 1) * 32 + (u & 1) * 4) * 288 + ((t == 4 && w2) ? 3 : t) * 64));
    u32 offA, offB;
    {
      const u32 cqA = c4 * 36 + (w2 + 2 * lr) * 4 + q4, cqB = c4 * 36 + 32 + q4;
      offA = p4 == 0 ? WS_FIN + (((cqA >> 1) * 2) * 64 + r) * 32 + (cqA & 1) * 16 : OOB;
      offB = (w2 == 0 && lr == 0 && p4 == 0) ? WS_FIN + (((cqB >> 1) * 2) * 64 + r) * 32 + (cqB & 1) * 16 : OOB;
      asm volatile("" : "+v"(offA), "+v"(offB));
    }
#pragma unroll
    for (int t = 0; t < 5; ++t)
#pragma unroll
      for (int u = 0; u < 4; ++u) asm volatile("" : "+v"(bb[t][u]));
    wait_flag_tight(FL_O, 1);
    STAMP(4);
    const s16x8 a0 = *(const LAS s16x8*)(smem + L_OI + lr * 16), a1 = *(const LAS s16x8*)(smem + L_OI + 64 + lr * 16);
    f32x4 acc[5];
#pragma unroll
    for (int t = 0; t < 5; ++t) {
      f32x4 c = {0.f, 0.f, 0.f, 0.f};
      c = __builtin_amdgcn_mfma_f32_16x16x32_bf16(__builtin_bit_cast(bf16x8, a0), __builtin_bit_cast(bf16x8, __builtin_shufflevector(bb[t][0], bb[t][1], 0, 1, 2, 3, 4, 5, 6, 7)), c, 0, 0, 0);
      c = __builtin_amdgcn_mfma_f32_16x16x32_bf16(__builtin_bit_cast(bf16x8, a1), __builtin_bit_cast(bf16x8, __builtin_shufflevector(bb[t][2], bb[t][3], 0, 1, 2, 3, 4, 5, 6, 7)), c, 0, 0, 0);
      acc[t] = c;
    }
    st_quad(rws, offA, quad_of_lanes(sel4(lr, acc[0][0], acc[1][0], acc[2][0], acc[3][0]), tag));
    if (w2 == 0) st_quad(rws, offB, quad_of_lanes(acc[4][0], tag));
    add_flag(FL_FIN);
    STAMP(5);
  } else if (wave == 10) {
    __builtin_amdgcn_s_setprio(1);
    const int r = j >> 2, c4 = j & 3;
    const rsrc_t rw = mk(a.wd, F * D * 2);
    const rsrc_t rws = mk(a.ws, WS_END);
    u32 vo[21];
#pragma unroll
    for (int i = 0; i < 21; ++i) {
      const u32 id = i * 64 + lane;
      const u32 row = (id * 3641u) >> 16, c = id - row * 18;
      const u32 grow = row >= 64 ? 4096 + 8 * r + (row - 64) : 64 * r + row;
      vo[i] = id < 1296u ? grow * 1152 + c4 * 288 + c * 16 : OOB;
      asm volatile("" : "+v"(vo[i]));
    }
    const Tag tag = gettag();
    wait_flag(FL_G0, 3);
    wait_flag(FL_MX, 4);
    STAMP(1);
#pragma unroll
    for (int i = 0; i < 21; ++i) dma16(rw, vo[i], 0, L_K + i * 1024);
    __builtin_amdgcn_s_setprio(3);
    STAMP(2);
    VMCNT(DH_LEFT);
    *flg(FL_DH) = 1u;
    VMCNT(0);
    set_flag(FL_DL);
    STAMP(3);
    down_gemv(0, j, lane, tag, rws);
    STAMP(5);
  } else {
    __builtin_amdgcn_s_setprio(3);
    const rsrc_t rws = mk(a.ws, WS_END);
    const int h = j >> 3, hf = (j >> 2) & 1;
    const Tag tag = gettag();
    const u32 lr = lane >> 4;
    {
      const u32 base = WS_Q + (j & ~15) * 256 + lane * 16;
      wait_flag(FL_QPUB, 1);
      __builtin_amdgcn_s_sleep(Q_DELAY);
      wait_flag(FL_VH, 1);
      STAMP(1);
      v4u g[4];
      GATHER(g, 4, base + i * 1024);
      f32x4 v[4];
#pragma unroll
      for (int i = 0; i < 4; ++i) v[i] = gv4(g[i]);
      float qv[4];
#pragma unroll
      for (int k = 0; k < 4; ++k) qv[k] = swap16_add(swap32_add(v[0][k], v[2][k]), swap32_add(v[1][k], v[3][k]));
      *(LAS v2u*)(smem + L_QI + (lr * 64 + (lane & 15) * 4) * 2) = v2u{pk(qv[0], qv[1]), pk(qv[2], qv[3])};
      set_flag(FL_Q);
      STAMP(2);
    }
    {
      const u32 bo = WS_OP + ((h * 16 + lr) * 32 + hf * 16 + (lane & 15)) * 16;
      const u32 bl = WS_ML + (h * 16 + ((lane & 12) | ((lane + lr) & 3))) * 16;
      wait_flag(FL_OPUB, 1);
      __builtin_amdgcn_s_sleep(O_DELAY);
      wait_flag(FL_DH, 1);
      STAMP(5);
      v4u g[5];
      GATHER(g, 5, (i == 0 ? bl : bo + (i - 1) * 2048));
      STAMP(6);
      const float m = gv(g[0].x), l = gv(g[0].y);
      float M = m; M = fmaxf(M, dpp<DPP_XOR1>(M)); M = fmaxf(M, dpp<DPP_XOR2>(M)); M = fmaxf(M, dpp<DPP_HMIRROR>(M)); M = fmaxf(M, dpp<DPP_MIRROR>(M));
      const float wt = __builtin_amdgcn_exp2f(m - M);
      float L = wt * l; L += dpp<DPP_XOR1>(L); L += dpp<DPP_XOR2>(L); L += dpp<DPP_HMIRROR>(L); L += dpp<DPP_MIRROR>(L);
      f32x4 acc = dpp<0x150>(wt) * gv4(g[1]);
      acc += dpp<0x154>(wt) * gv4(g[2]);
      acc += dpp<0x158>(wt) * gv4(g[3]);
      acc += dpp<0x15C>(wt) * gv4(g[4]);
      const float o = swap16_add(swap32_add(acc[0], acc[2]), swap32_add(acc[1], acc[3])) * __builtin_amdgcn_rcpf(L);
      ((LAS bf16_t*)(smem + L_OI))[(lane & 15) * 4 + lr] = (bf16_t)(pk(o, o) & 0xFFFFu);
      set_flag(FL_O);
      STAMP(7);
    }
  }
  STAMP(9);
  STAMP_FLUSH();
}

}

extern "C" void kernel_launch(void* const* d_in, const int* in_sizes, int n_in, void* d_out, int out_size, void* d_ws, size_t ws_size,
                              hipStream_t stream) {
  Args a;
  a.x = (const bf16_t*)d_in[0]; a.kc = (const bf16_t*)d_in[1]; a.vc = (const bf16_t*)d_in[2];
  a.wq = (const bf16_t*)d_in[3]; a.wo = (const bf16_t*)d_in[4];
  a.wg = (const bf16_t*)d_in[6]; a.wu = (const bf16_t*)d_in[7]; a.wd = (const bf16_t*)d_in[8];
  a.out = (bf16_t*)d_out; a.ws = (unsigned char*)d_ws;
  (void)hipFuncSetAttribute((const void*)fused_kernel, hipFuncAttributeMaxDynamicSharedMemorySize, L_END);
  fused_kernel<<<256, NTHR, L_END, stream>>>(a);
}
```

```cpp
#define LDPOL 18
#include <hip/hip_runtime.h>
#include <stdint.h>

typedef unsigned short bf16_t;
typedef unsigned u32;
typedef unsigned long long u64;
typedef u32 v4u __attribute__((ext_vector_type(4)));
typedef u32 v2u __attribute__((ext_vector_type(2)));
typedef float f32x4 __attribute__((ext_vector_type(4)));
typedef short s16x4 __attribute__((ext_vector_type(4)));
typedef short s16x8 __attribute__((ext_vector_type(8)));
typedef __bf16 bf16x2 __attribute__((ext_vector_type(2)));
typedef __bf16 bf16x8 __attribute__((ext_vector_type(8)));
#define LAS __attribute__((address_space(3)))
#define GAS __attribute__((address_space(1)))
typedef __amdgpu_buffer_rsrc_t rsrc_t;

namespace {

constexpr int D = 576, SK = 65536, DH = 128, WIN = 4096, F = 4608, NQ = 4096;
constexpr int NWAVES = 12, NTHR = NWAVES * 64;

constexpr u32 WS_EPOCH = 0;
constexpr u32 WS_Q = 4096;
constexpr u32 WS_GM = WS_Q + 256 * 256;
constexpr u32 WS_GP = WS_GM + 64 * 8 * 256;
constexpr u32 WS_OP = WS_GP + 256 * 256;
constexpr u32 WS_ML = WS_OP + 32 * 16 * 512;
constexpr u32 WS_FIN = WS_ML + 32 * 16 * 16;
constexpr u32 WS_END = WS_FIN + 72 * 2 * 64 * 32;
constexpr u32 WS_STAMP = 4u << 20;

constexpr u32 L_K = 0;
constexpr u32 L_V = 65536;
constexpr u32 L_WO = 131072;
constexpr u32 L_XN = L_WO + 18432;
constexpr u32 L_QI = L_XN + 2304;
constexpr u32 L_PI = L_QI + 512;
constexpr u32 L_OW = L_PI + 1024;
constexpr u32 L_HI = L_OW + 4096;
constexpr u32 L_OI = L_HI + 160;
constexpr u32 L_WT = L_OI + 128;
constexpr u32 L_MX = L_WT + 64;
constexpr u32 L_TAG = L_MX + 64;
constexpr u32 L_FLG = L_TAG + 32;
constexpr u32 L_END = L_FLG + 128;
static_assert(L_END <= 163840, "LDS");
enum { FL_Q, FL_VISS, FL_MX, FL_OW, FL_OPUB, FL_H, FL_O, FL_WOL, FL_LS, FL_FIN, FL_G0, FL_TAG, FL_W0, FL_M2, FL_QPUB, FL_HPUB, FL_DL, FL_VH, FL_DH };

#define DMAPOL "sc1 nt"
constexpr int AUX_NT = LDPOL, AUX_SC1 = 16;
constexpr u32 SPIN_CAP = 1u << 22;
constexpr u32 OOB = 0x7FFFFFF0u;
#define KVROT 1
#define KVTILE(t, s) ((t) * 16 + (((s) + KVROT * (t)) & 15))
#define Q_DELAY 4
#define H_DELAY 8
#define ML_DELAY 12
#define O_DELAY 8
#define F_DELAY 10
#define DH_LEFT 10
#define VH_AT 10

extern __shared__ __attribute__((aligned(16))) unsigned char smem[];

__device__ __forceinline__ float lo2f(u32 p) { return __uint_as_float(p << 16); }
__device__ __forceinline__ float hi2f(u32 p) { return __uint_as_float(p & 0xFFFF0000u); }
__device__ __forceinline__ u32 pk(float a, float b) {
  bf16x2 r = {(__bf16)a, (__bf16)b};
  return __builtin_bit_cast(u32, r);
}
__device__ __forceinline__ float rbf(float f) { return lo2f(pk(f, f)); }
__device__ __forceinline__ float sel(bool c, float a, float b) { return c ? a : b; }
__device__ __forceinline__ float sel4(u32 g, float a, float b, float c, float d) {
  const bool b0 = g & 1, b1 = g & 2;
  const float lo = b0 ? b : a, hi = b0 ? d : c;
  return b1 ? hi : lo;
}
__device__ __forceinline__ rsrc_t mk(const void* p, u32 bytes) {
  return __builtin_amdgcn_make_buffer_rsrc((void*)p, 0, bytes, 0x00020000);
}
__device__ __forceinline__ float dot2(u32 w, u32 x, float acc) {
  return __builtin_amdgcn_fdot2_f32_bf16(__builtin_bit_cast(bf16x2, w), __builtin_bit_cast(bf16x2, x), acc, false);
}
template <int CTRL> __device__ __forceinline__ float dpp(float x) {
  return __builtin_bit_cast(float, __builtin_amdgcn_mov_dpp(__builtin_bit_cast(int, x), CTRL, 0xf, 0xf, true));
}
constexpr int DPP_XOR1 = 0xB1, DPP_XOR2 = 0x4E, DPP_HMIRROR = 0x141, DPP_MIRROR = 0x140, DPP_ROR4 = 0x124, DPP_ROR8 = 0x128;
__device__ __forceinline__ float swap16_add(float a, float b) {
  auto s = __builtin_amdgcn_permlane16_swap(__float_as_uint(a), __float_as_uint(b), false, false);
  return __uint_as_float(s[0]) + __uint_as_float(s[1]);
}
__device__ __forceinline__ float swap32_add(float a, float b) {
  auto s = __builtin_amdgcn_permlane32_swap(__float_as_uint(a), __float_as_uint(b), false, false);
  return __uint_as_float(s[0]) + __uint_as_float(s[1]);
}
__device__ __forceinline__ float swap16_max(float a) {
  auto s = __builtin_amdgcn_permlane16_swap(__float_as_uint(a), __float_as_uint(a), false, false);
  return fmaxf(__uint_as_float(s[0]), __uint_as_float(s[1]));
}
__device__ __forceinline__ float swap32_max(float a) {
  auto s = __builtin_amdgcn_permlane32_swap(__float_as_uint(a), __float_as_uint(a), false, false);
  return fmaxf(__uint_as_float(s[0]), __uint_as_float(s[1]));
}
__device__ __forceinline__ float wave_sum(float x) {
  x += dpp<DPP_XOR1>(x); x += dpp<DPP_XOR2>(x); x += dpp<DPP_HMIRROR>(x); x += dpp<DPP_MIRROR>(x);
  x = swap16_add(x, x); x = swap32_add(x, x);
  return x;
}
__device__ __forceinline__ float wave_max(float x) {
  x = fmaxf(x, dpp<DPP_XOR1>(x)); x = fmaxf(x, dpp<DPP_XOR2>(x)); x = fmaxf(x, dpp<DPP_HMIRROR>(x)); x = fmaxf(x, dpp<DPP_MIRROR>(x));
  x = swap16_max(x); x = swap32_max(x);
  return x;
}

struct Tag { u32 t0, t1, t2, t3; };
__device__ __forceinline__ u32 q22(float v, u32 t) { return ((__float_as_uint(v) + 0x200u) & 0xFFFFFC00u) | t; }
__device__ __forceinline__ float r22(float v) { return __uint_as_float((__float_as_uint(v) + 0x200u) & 0xFFFFFC00u); }
__device__ __forceinline__ v4u quad(float a, float b, float c, float d, Tag t) { return v4u{q22(a, t.t0), q22(b, t.t1), q22(c, t.t2), q22(d, t.t3)}; }
__device__ __forceinline__ float gv(u32 d) { return __uint_as_float(d & 0xFFFFFC00u); }
__device__ __forceinline__ f32x4 gv4(v4u v) { return f32x4{gv(v.x), gv(v.y), gv(v.z), gv(v.w)}; }
struct Chk {
  u32 a = 0;
  __device__ __forceinline__ void add(v4u v, Tag t) {
    a = __builtin_amdgcn_bitop3_b32(v.x, t.t0, a, 0xBE); a = __builtin_amdgcn_bitop3_b32(v.y, t.t1, a, 0xBE);
    a = __builtin_amdgcn_bitop3_b32(v.z, t.t2, a, 0xBE); a = __builtin_amdgcn_bitop3_b32(v.w, t.t3, a, 0xBE);
  }
  __device__ __forceinline__ bool ok() const { return __all((a & 0x3FFu) == 0u); }
};
__device__ __forceinline__ void st_quad(rsrc_t rs, u32 off, v4u q) { __builtin_amdgcn_raw_buffer_store_b128(q, rs, off, 0, AUX_SC1); }
__device__ __forceinline__ v4u ld_quad(rsrc_t rs, u32 off) { return __builtin_amdgcn_raw_buffer_load_b128(rs, off, 0, AUX_SC1); }
#define GATHER(g, N, OFFEXPR)                                              \
  _Pragma("unroll 1") for (u32 sp_ = 0; sp_ < SPIN_CAP; ++sp_) {          \
    asm volatile("" ::: "memory");                                         \
    Chk c_;                                                                \
    _Pragma("unroll") for (int i = 0; i < N; ++i) g[i] = ld_quad(rws, OFFEXPR); \
    _Pragma("unroll") for (int i = 0; i < N; ++i) c_.add(g[i], tag);      \
    COUNT_ATTEMPT();                                                       \
    if (c_.ok()) break;                                                    \
  }

__device__ __forceinline__ volatile LAS u32* flg(int i) { return (volatile LAS u32*)(smem + L_FLG) + i; }
__device__ __forceinline__ void wait_flag(int i, u32 want) {
  volatile LAS u32* p = flg(i);
#pragma unroll 1
  for (u32 s = 0; s < SPIN_CAP; ++s) {
    if (__builtin_amdgcn_readfirstlane(*p) >= want) break;
    __builtin_amdgcn_s_sleep(1);
  }
  asm volatile("" ::: "memory");
}
__device__ __forceinline__ void wait_flag_tight(int i, u32 want) {
  volatile LAS u32* p = flg(i);
#pragma unroll 1
  for (u32 s = 0; s < SPIN_CAP; ++s) {
    if (__builtin_amdgcn_readfirstlane(*p) >= want) break;
  }
  asm volatile("" ::: "memory");
}
__device__ __forceinline__ Tag gettag() {
  wait_flag(FL_TAG, 1);
  const v4u tg = *(const LAS v4u*)(smem + L_TAG);
  Tag t{(u32)__builtin_amdgcn_readfirstlane(tg.x), (u32)__builtin_amdgcn_readfirstlane(tg.y), (u32)__builtin_amdgcn_readfirstlane(tg.z), (u32)__builtin_amdgcn_readfirstlane(tg.w)};
  asm volatile("" : "+s"(t.t0), "+s"(t.t1), "+s"(t.t2), "+s"(t.t3));
  return t;
}
__device__ __forceinline__ void set_flag(int i) {
  asm volatile("s_waitcnt lgkmcnt(0)" ::: "memory");
  *flg(i) = 1u;
  asm volatile("" ::: "memory");
}
__device__ __forceinline__ void add_flag(int i) {
  asm volatile("s_waitcnt lgkmcnt(0)" ::: "memory");
  if ((threadIdx.x & 63) == 0) __hip_atomic_fetch_add((LAS u32*)(smem + L_FLG) + i, 1u, __ATOMIC_RELAXED, __HIP_MEMORY_SCOPE_WORKGROUP);
  asm volatile("" ::: "memory");
}
__device__ __forceinline__ u32 bump_flag(int i) {
  asm volatile("s_waitcnt lgkmcnt(0)" ::: "memory");
  u32 r = 0;
  if ((threadIdx.x & 63) == 0) r = __hip_atomic_fetch_add((LAS u32*)(smem + L_FLG) + i, 1u, __ATOMIC_RELAXED, __HIP_MEMORY_SCOPE_WORKGROUP);
  r = __builtin_amdgcn_readfirstlane(r);
  asm volatile("" ::: "memory");
  return r;
}
__device__ __forceinline__ void dma16(rsrc_t rs, u32 voff, u32 soff, u32 ldsoff) {
  asm volatile("s_mov_b32 m0, %0\n\ts_nop 0\n\tbuffer_load_dwordx4 %1, %2, %3 offen " DMAPOL " lds" ::"s"(ldsoff), "v"(voff), "s"(rs), "s"(soff) : "m0", "memory");
}
#define COUNT_ATTEMPT() do { } while (0)
#define VMCNT_(n) asm volatile("s_waitcnt vmcnt(" #n ")" ::: "memory")
#define VMCNT(n) VMCNT_(n)
#define SB() __builtin_amdgcn_sched_barrier(0)
#define BARRIER() do { __builtin_amdgcn_sched_barrier(0); asm volatile("s_waitcnt lgkmcnt(0)" ::: "memory"); __builtin_amdgcn_s_barrier(); asm volatile("" ::: "memory"); __builtin_amdgcn_sched_barrier(0); } while (0)

#define STAMP(i) do { } while (0)
#define STAMP_FLUSH() do { } while (0)

struct Args {
  const bf16_t* x; const bf16_t* kc; const bf16_t* vc; const bf16_t* wq; const bf16_t* wo;
  const bf16_t* wg; const bf16_t* wu; const bf16_t* wd; bf16_t* out; unsigned char* ws;
};

__device__ __forceinline__ float silu_b(float v) {
  return rbf(v * __builtin_amdgcn_rcpf(1.f + __builtin_amdgcn_exp2f(-1.4426950408889634f * v)));
}
__device__ __forceinline__ v4u quad_of_lanes(float v, Tag tag) {
  return quad(dpp<0x00>(v), dpp<0x55>(v), dpp<0xAA>(v), dpp<0xFF>(v), tag);
}

__device__ __forceinline__ void down_gemv(int part, int j, int lane, Tag tag, rsrc_t rws) {
  const int r = j >> 2, c4 = j & 3;
  const u32 lr = lane >> 4, q4 = (lane & 15) >> 2, p4 = lane & 3;
  const u32 badr = L_K + (8 * lr + q4) * 288 + p4 * 8 + part * 96;
  const u32 row4 = 64 + 4 * lr + q4;
  const u32 badr4 = L_K + (row4 < 72 ? row4 : 71) * 288 + p4 * 8 + part * 96;
  const u32 cq = c4 * 36 + (3 * part + lr) * 4 + q4;
  u32 off = (lr < 3 && p4 == 0) ? WS_FIN + (((cq >> 1) * 2 + 1) * 64 + r) * 32 + (cq & 1) * 16 : OOB;
  asm volatile("" : "+v"(off));
  wait_flag(FL_DL, 1);
  s16x4 b[3][5];
#pragma unroll
  for (int t = 0; t < 3; ++t) {
#pragma unroll
    for (int u = 0; u < 4; ++u) b[t][u] = __builtin_amdgcn_ds_read_tr16_b64_v4i16((LAS s16x4*)(smem + badr + ((u >> 1) * 32 + (u & 1) * 4) * 288 + t * 32));
    b[t][4] = __builtin_amdgcn_ds_read_tr16_b64_v4i16((LAS s16x4*)(smem + badr4 + t * 32));
  }
#pragma unroll
  for (int t = 0; t < 3; ++t)
#pragma unroll
    for (int u = 0; u < 5; ++u) asm volatile("" : "+v"(b[t][u]));
  wait_flag_tight(FL_H, 1);
  const s16x8 a0 = *(const LAS s16x8*)(smem + L_HI + lr * 16), a1 = *(const LAS s16x8*)(smem + L_HI + 64 + lr * 16);
  const s16x4 a2 = *(const LAS s16x4*)(smem + L_HI + 128 + lr * 8);
  f32x4 acc[3];
#pragma unroll
  for (int t = 0; t < 3; ++t) {
    f32x4 c = {0.f, 0.f, 0.f, 0.f};
    c = __builtin_amdgcn_mfma_f32_16x16x16bf16_1k(a2, b[t][4], c, 0, 0, 0);
    c = __builtin_amdgcn_mfma_f32_16x16x32_bf16(__builtin_bit_cast(bf16x8, a0), __builtin_bit_cast(bf16x8, __builtin_shufflevector(b[t][0], b[t][1], 0, 1, 2, 3, 4, 5, 6, 7)), c, 0, 0, 0);
    c = __builtin_amdgcn_mfma_f32_16x16x32_bf16(__builtin_bit_cast(bf16x8, a1), __builtin_bit_cast(bf16x8, __builtin_shufflevector(b[t][2], b[t][3], 0, 1, 2, 3, 4, 5, 6, 7)), c, 0, 0, 0);
    acc[t] = c;
  }
  st_quad(rws, off, quad_of_lanes(sel4(lr, acc[0][0], acc[1][0], acc[2][0], acc[2][0]), tag));
  add_flag(FL_FIN);
}

__global__ void __launch_bounds__(NTHR, 3) fused_kernel(Args a) {
  const int tid = threadIdx.x, lane = tid & 63;
  const int wave = __builtin_amdgcn_readfirstlane(tid >> 6);
  const int b = blockIdx.x;
  const int j = (b & 7) * 32 + (b >> 3);
  STAMP(0);
  if (wave == 11) {
    if (lane < 32) *flg(lane) = 0u;
    if (lane >= 32 && lane < 36) *(LAS u32*)(smem + L_HI + 144 + (lane - 32) * 4) = 0u;
  }
  BARRIER();

  if (__builtin_expect(wave < 4, 1)) {
    __builtin_amdgcn_s_setprio(3);
    const int kp = j & 3, r = j >> 2;
    const bf16_t* wm; u32 row0, stride, strip, nrows, pub;
    if (wave == 0) { wm = a.wq; row0 = 144 * kp; stride = NQ * 2; strip = r; nrows = 144; pub = WS_Q + j * 256; }
    else if (wave < 3) {
      const int quarter = 2 * (kp & 1) + (wave - 1);
      wm = (kp >> 1) ? a.wu : a.wg; row0 = 144 * quarter; stride = F * 2; nrows = 144;
      strip = (r & ~7) | ((r + 3 * wave) & 7);
      pub = WS_GM + ((strip * 2 + (kp >> 1)) * 4 + quarter) * 256;
    } else { wm = ((j >> 4) & 1) ? a.wu : a.wg; row0 = 36 * (j & 15); stride = F * 2; strip = 64 + (j >> 5); nrows = 36; pub = WS_GP + j * 256; }
    const rsrc_t rw = mk((const unsigned char*)wm + (size_t)row0 * stride + strip * 128, (nrows - 1) * stride + 128);
    const rsrc_t rx = mk(a.x, D * 2);
    const rsrc_t rws = mk(a.ws, WS_END);
    const u64 ep = __hip_atomic_load((GAS u64*)(a.ws + WS_EPOCH), __ATOMIC_RELAXED, __HIP_MEMORY_SCOPE_AGENT);
    u32 xr[5];
#pragma unroll
    for (int i = 0; i < 5; ++i) xr[i] = __builtin_amdgcn_raw_buffer_load_b32(rx, lane * 4, i * 256, 0);
    const u32 rs8 = lane >> 3;
    const u32 voff = rs8 * stride + (lane & 7) * 16;
    u64 tg = ep;
    Tag tag;
    auto layernorm = [&](v4u (&av)[18]) {
#pragma unroll
      for (int i = 0; i < 5; ++i) asm volatile("" : "+v"(xr[i]));
      asm volatile("" : "+v"(tg));
      tg += 1;
      {
        const u64 T = ((u64)(u32)__builtin_amdgcn_readfirstlane((u32)(tg >> 32)) << 32) | (u32)__builtin_amdgcn_readfirstlane((u32)tg);
        const u64 H = T * 0x9E3779B97F4A7C15ull;
        tag = Tag{(u32)T & 0x3FFu, (u32)(H >> 54), (u32)(H >> 44) & 0x3FFu, (u32)(H >> 34) & 0x3FFu};
        if (wave == 0) {
          if (lane == 0) { *(LAS v4u*)(smem + L_TAG) = v4u{tag.t0, tag.t1, tag.t2, tag.t3}; *(LAS v2u*)(smem + L_TAG + 16) = v2u{(u32)T, (u32)(T >> 32)}; }
          set_flag(FL_TAG);
        }
      }
      float s = 0.f, q = 0.f;
#pragma unroll
      for (int i = 0; i < 5; ++i) { s = dot2(xr[i], 0x3F803F80u, s); q = dot2(xr[i], xr[i], q); }
      s = wave_sum(s); q = wave_sum(q);
      const float mean = s * (1.f / 576.f);
      const float var = rbf(fmaxf(fmaf(-mean, mean, q * (1.f / 576.f)), 0.f));
      const float mb = rbf(mean);
      const float inv = __builtin_amdgcn_rcpf(rbf(__builtin_amdgcn_sqrtf(rbf(var + 1e-5f))));
      LAS v2u* xn = (LAS v2u*)(smem + L_XN);
#pragma unroll
      for (int i = 0; i < 5; ++i) {
        const u32 d = pk(lo2f(xr[i]) - mb, hi2f(xr[i]) - mb);
        const u32 n = pk(lo2f(d) * inv, hi2f(d) * inv);
        if (i < 4 || lane < 32) xn[i * 64 + lane] = v2u{__builtin_amdgcn_perm(n, n, 0x01000100u), __builtin_amdgcn_perm(n, n, 0x03020302u)};
      }
      asm volatile("s_waitcnt lgkmcnt(0)" ::: "memory");
      u32 xp[18];
      const LAS u32* xs = (const LAS u32*)(smem + L_XN) + row0 + rs8;
#pragma unroll
      for (int i = 0; i < 18; ++i) xp[i] = xs[(wave == 3 && i > 4) ? 0 : 8 * i];
      if (wave == 3 && rs8 >= 4) xp[4] = 0u;
      u32 msk[4];
#pragma unroll
      for (int d = 0; d < 4; ++d) msk[d] = (((lane & 7) >> 1) == d) ? ((lane & 1) ? 0xFFFF0000u : 0x0000FFFFu) : 0u;
#pragma unroll
      for (int i = 0; i < 18; ++i) av[i] = v4u{xp[i] & msk[0], xp[i] & msk[1], xp[i] & msk[2], xp[i] & msk[3]};
    };
    auto gemv_publish = [&](const v4u (&av)[18], const v4u (&w)[18]) {
      f32x4 da = {0.f, 0.f, 0.f, 0.f}, db = {0.f, 0.f, 0.f, 0.f};
#pragma unroll
      for (int i = 0; i < 18; ++i) {
        if (i & 1) db = __builtin_amdgcn_mfma_f32_16x16x32_bf16(__builtin_bit_cast(bf16x8, av[i]), __builtin_bit_cast(bf16x8, w[i]), db, 0, 0, 0);
        else da = __builtin_amdgcn_mfma_f32_16x16x32_bf16(__builtin_bit_cast(bf16x8, av[i]), __builtin_bit_cast(bf16x8, w[i]), da, 0, 0, 0);
      }
      da += db;
      f32x4 res;
#pragma unroll
      for (int k = 0; k < 4; ++k) {
        const float up = dpp<DPP_ROR8>(da[k]);
        res[k] = da[k] + __uint_as_float(__builtin_amdgcn_permlane32_swap(__float_as_uint(up), __float_as_uint(up), false, false)[1]);
      }
      const u32 off = ((lane & 0x28) == 0) ? pub + (2 * (lane & 7) + (lane >> 4)) * 16 : OOB;
      st_quad(rws, off, quad(res[0], res[1], res[2], res[3], tag));
    };
    if (__builtin_expect(wave == 0, 1)) {
      v4u w[18], av[18];
#pragma unroll
      for (int i = 0; i < 18; ++i) w[i] = __builtin_amdgcn_raw_buffer_load_b128(rw, voff, 8 * i * stride, AUX_NT);
      SB();
      set_flag(FL_W0);
      STAMP(1);
      layernorm(av);
      STAMP(2);
      gemv_publish(av, w);
      set_flag(FL_QPUB);
      STAMP(3);
      {
        const u32 bm = WS_GM + r * 2048 + lane * 16;
        const u32 bp = WS_GP + ((r >> 3) * 32 + (lane >> 1)) * 256 + ((r & 7) * 2 + (lane & 1)) * 16;
        wait_flag(FL_HPUB, 3);
        __builtin_amdgcn_s_sleep(H_DELAY);
        wait_flag(FL_DH, 1);
        STAMP(4);
        v4u g[3];
        GATHER(g, 3, (i < 2 ? bm + i * 1024 : bp));
        STAMP(5);
        const f32x4 gg = gv4(g[0]), uu = gv4(g[1]), tt = gv4(g[2]);
        const bool tail = lane >= 16;
        float hv[4];
#pragma unroll
        for (int k = 0; k < 4; ++k) {
          float m = swap16_add(gg[k], uu[k]);
          m = swap32_add(m, m);
          float t = tt[k];
          t += dpp<DPP_XOR2>(t); t += dpp<DPP_ROR4>(t); t += dpp<DPP_ROR8>(t); t = swap16_add(t, t);
          const auto sm = __builtin_amdgcn_permlane16_swap(__float_as_uint(m), __float_as_uint(m), false, false);
          const auto stl = __builtin_amdgcn_permlane32_swap(__float_as_uint(t), __float_as_uint(t), false, false);
          const float gs = __uint_as_float(tail ? stl[0] : sm[0]), us = __uint_as_float(tail ? stl[1] : sm[1]);
          hv[k] = silu_b(rbf(gs)) * rbf(us);
        }
        if (lane < 18) *(LAS v2u*)(smem + L_HI + lane * 8) = v2u{pk(hv[0], hv[1]), pk(hv[2], hv[3])};
        set_flag(FL_H);
        STAMP(6);
      }
      if ((j & 31) < 9) {
        const int f = (j >> 5) * 9 + (j & 31);
        const u32 mycol = f * 8 + 4 * (lane & 1) + 2 * ((lane >> 4) & 1) + ((lane >> 3) & 1);
        const u32 xo = __builtin_amdgcn_raw_buffer_load_b16(rx, mycol * 2, 0, 0);
        const u32 base = WS_FIN + f * 4096 + lane * 16;
        wait_flag(FL_FIN, 5);
        __builtin_amdgcn_s_sleep(F_DELAY);
        STAMP(7);
        v4u g[4];
        GATHER(g, 4, base + i * 1024);
        STAMP(1);
        const f32x4 sv = gv4(g[0]) + gv4(g[1]), fv = gv4(g[2]) + gv4(g[3]);
        float a4[4];
#pragma unroll
        for (int k = 0; k < 4; ++k) a4[k] = swap32_add(sv[k], fv[k]);
        const float a0 = swap16_add(a4[0], a4[2]), a1 = swap16_add(a4[1], a4[3]);
        float b0 = a0 + dpp<DPP_XOR2>(a0), b1 = a1 + dpp<DPP_XOR2>(a1);
        b0 += dpp<DPP_ROR4>(b0); b1 += dpp<DPP_ROR4>(b1);
        b0 += dpp<DPP_ROR8>(b0); b1 += dpp<DPP_ROR8>(b1);
        const float t = (lane & 8) ? b1 : b0;
        const auto sf = __builtin_amdgcn_permlane32_swap(__float_as_uint(t), __float_as_uint(t), false, false);
        const float sb = rbf(__uint_as_float(sf[0])), fb = rbf(__uint_as_float(sf[1]));
        const float o = lo2f(xo) + rbf(sb * silu_b(fb));
        if ((lane & 0x26) == 0) *(GAS bf16_t*)((GAS unsigned char*)a.out + mycol * 2) = (bf16_t)(pk(o, o) & 0xFFFFu);
        if (f == 0 && lane == 0) { const v2u nx = *(const LAS v2u*)(smem + L_TAG + 16); __hip_atomic_store((GAS u64*)(a.ws + WS_EPOCH), (u64)nx.x | ((u64)nx.y << 32), __ATOMIC_RELAXED, __HIP_MEMORY_SCOPE_AGENT); }
        STAMP(2);
      }
    } else {
      v4u w[18], av[18];
      layernorm(av);
      STAMP(2);
      __builtin_amdgcn_s_setprio(1);
#pragma unroll
      for (int i = 0; i < 18; ++i) asm volatile("" : "+v"(av[i]));
      wait_flag(FL_VISS, 4);
#pragma unroll
      for (int i = 0; i < 18; ++i) w[i] = __builtin_amdgcn_raw_buffer_load_b128(rw, voff, 8 * i * stride, AUX_NT);
      SB();
      add_flag(FL_G0);
      __builtin_amdgcn_s_setprio(3);
      STAMP(5);
      gemv_publish(av, w);
      add_flag(FL_HPUB);
      STAMP(3);
      if (wave >= 2) {
        down_gemv(wave - 1, j, lane, tag, rws);
        STAMP(6);
      }
    }
  } else if (__builtin_expect(wave < 8, 1)) {
    __builtin_amdgcn_s_setprio(0);
    const int w = wave - 4, g = j >> 4, s = j & 15;
    const rsrc_t rk = mk(a.kc + ((size_t)g * SK + (SK - WIN)) * DH, WIN * DH * 2);
    const rsrc_t rv = mk(a.vc + ((size_t)g * SK + (SK - WIN)) * DH, WIN * DH * 2);
    const rsrc_t rws = mk(a.ws, WS_END);
    const u32 lr = lane >> 4;
    u32 vo[4];
#pragma unroll
    for (int sub = 0; sub < 4; ++sub) { vo[sub] = sub * 1024 + lr * 256 + ((((lane & 15) ^ (lr << 2)) ^ sub) << 4); asm volatile("" : "+v"(vo[sub])); }
    wait_flag(FL_W0, 1);
#pragma unroll
    for (int p = 0; p < 16; ++p) dma16(rk, vo[p & 3], KVTILE(4 * w + (p >> 2), s) * 4096, L_K + w * 16384 + p * 1024);
#pragma unroll
    for (int p = 0; p < 16; ++p) {
      dma16(rv, vo[p & 3], KVTILE(4 * w + (p >> 2), s) * 4096, L_V + w * 16384 + p * 1024);
      if (p == VH_AT && w == 0) *flg(FL_VH) = 1u;
    }
    add_flag(FL_VISS);
    __builtin_amdgcn_s_setprio(3);
    STAMP(1);
    const Tag tag = gettag();
    const u32 krow = lane & 15;
    u32 kadr = L_K + w * 16384 + krow * 256 + ((lr ^ (((krow & 3) << 2) | ((krow >> 2) & 3))) << 4);
    const u32 q4 = (lane & 15) >> 2, p4 = lane & 3;
    u32 va[2][8];
#pragma unroll
    for (int t = 0; t < 2; ++t) {
      const u32 row = 8 * (lr & 1) + 4 * t + q4;
      const u32 base = L_V + w * 16384 + (lr >> 1) * 4096 + row * 256 + (((p4 >> 1) ^ ((q4 << 2) | (2 * (lr & 1) + t))) << 4) + 8 * (p4 & 1);
#pragma unroll
      for (int c = 0; c < 8; ++c) { va[t][c] = base ^ (c << 5); asm volatile("" : "+v"(va[t][c])); }
    }
    u32 ob = WS_OP + (((2 * g + (lane >> 5)) * 16 + s) * 32 + (lane & 31)) * 16;
    asm volatile("" : "+v"(kadr), "+v"(ob));
    VMCNT(16);
    s16x8 kf[16];
#pragma unroll
    for (int i = 0; i < 4; ++i)
#pragma unroll
      for (int st = 0; st < 4; ++st) kf[i * 4 + st] = *(const LAS s16x8*)(smem + ((kadr ^ (st << 6)) + i * 4096));
#pragma unroll
    for (int i = 0; i < 16; ++i) asm volatile("" : "+v"(kf[i]));
    add_flag(FL_MX);
    STAMP(2);
    wait_flag(FL_Q, 1);
    STAMP(3);
    s16x8 qf[4];
#pragma unroll
    for (int st = 0; st < 4; ++st) qf[st] = *(const LAS s16x8*)(smem + L_QI + (lane & 1) * 256 + st * 64 + lr * 16);
    f32x4 sc[4];
#pragma unroll
    for (int i = 0; i < 4; ++i) sc[i] = f32x4{0.f, 0.f, 0.f, 0.f};
#pragma unroll
    for (int st = 0; st < 4; ++st)
#pragma unroll
      for (int i = 0; i < 4; ++i)
        sc[i] = __builtin_amdgcn_mfma_f32_16x16x32_bf16(__builtin_bit_cast(bf16x8, qf[st]), __builtin_bit_cast(bf16x8, kf[i * 4 + st]), sc[i], 0, 0, 0);
    constexpr float C2 = 0.08838834764831845f * 1.4426950408889634f;
    const float s0 = sel4(lr, sc[0][0], sc[1][0], sc[2][0], sc[3][0]) * C2;
    const float s1 = sel4(lr, sc[0][1], sc[1][1], sc[2][1], sc[3][1]) * C2;
    LAS float* mx = (LAS float*)(smem + L_MX);
    {
      const float wm0 = wave_max(s0), wm1 = wave_max(s1);
      if (lane == 0) *(LAS v2u*)(mx + 2 * w) = v2u{__float_as_uint(wm0), __float_as_uint(wm1)};
    }
    add_flag(FL_M2);
    wait_flag_tight(FL_M2, 4);
    const f32x4 ma = *(const LAS f32x4*)(mx), mb4 = *(const LAS f32x4*)(mx + 4);
    const float m0 = r22(fmaxf(fmaxf(ma[0], ma[2]), fmaxf(mb4[0], mb4[2])));
    const float m1 = r22(fmaxf(fmaxf(ma[1], ma[3]), fmaxf(mb4[1], mb4[3])));
    const u32 pp = pk(__builtin_amdgcn_exp2f(s0 - m0), __builtin_amdgcn_exp2f(s1 - m1));
    LAS bf16_t* pi = (LAS bf16_t*)(smem + L_PI + w * 256);
    pi[lane] = (bf16_t)(pp & 0xFFFFu); pi[64 + lane] = (bf16_t)(pp >> 16);
    {
      const float l0 = wave_sum(lo2f(pp)), l1 = wave_sum(hi2f(pp));
      if (lane == 0) *(LAS v2u*)(mx + 8 + 2 * w) = v2u{__float_as_uint(l0), __float_as_uint(l1)};
    }
    s16x8 pf[2];
#pragma unroll
    for (int ks = 0; ks < 2; ++ks) pf[ks] = *(const LAS s16x8*)(smem + L_PI + w * 256 + (lane & 1) * 128 + ks * 64 + lr * 16);
    f32x4 oc[8];
#pragma unroll
    for (int c = 0; c < 8; ++c) oc[c] = f32x4{0.f, 0.f, 0.f, 0.f};
    STAMP(4);
#pragma unroll
    for (int ks = 0; ks < 2; ++ks) {
      if (ks == 0) VMCNT(8); else VMCNT(0);
      if (ks == 1) STAMP(5);
      s16x4 b0[8], b1[8];
#pragma unroll
      for (int c = 0; c < 8; ++c) {
        b0[c] = __builtin_amdgcn_ds_read_tr16_b64_v4i16((LAS s16x4*)(smem + va[0][c] + ks * 8192));
        b1[c] = __builtin_amdgcn_ds_read_tr16_b64_v4i16((LAS s16x4*)(smem + va[1][c] + ks * 8192));
      }
      SB();
#pragma unroll
      for (int c = 0; c < 8; ++c) {
        const s16x8 bb = __builtin_shufflevector(b0[c], b1[c], 0, 1, 2, 3, 4, 5, 6, 7);
        oc[c] = __builtin_amdgcn_mfma_f32_16x16x32_bf16(__builtin_bit_cast(bf16x8, pf[ks]), __builtin_bit_cast(bf16x8, bb), oc[c], 0, 0, 0);
      }
      SB();
    }
    if (lane < 16) {
      LAS float* ow = (LAS float*)(smem + L_OW) + w * 256 + lane;
#pragma unroll
      for (int c = 0; c < 8; ++c) { ow[c * 16] = oc[c][0]; ow[128 + c * 16] = oc[c][1]; }
    }
    const u32 arrived = bump_flag(FL_OW);
    STAMP(6);
    if (arrived == 3) {
      f32x4 t[4];
#pragma unroll
      for (int u = 0; u < 4; ++u) t[u] = *(const LAS f32x4*)(smem + L_OW + u * 1024 + lane * 16);
      const f32x4 la = *(const LAS f32x4*)(mx + 8), lb = *(const LAS f32x4*)(mx + 12);
      const f32x4 o = (t[0] + t[1]) + (t[2] + t[3]);
      st_quad(rws, ob, quad(o[0], o[1], o[2], o[3], tag));
      const float L = (lane >> 5) ? (la[1] + la[3]) + (lb[1] + lb[3]) : (la[0] + la[2]) + (lb[0] + lb[2]);
      st_quad(rws, (lane & 31) == 0 ? WS_ML + ((2 * g + (lane >> 5)) * 16 + s) * 16 : OOB, quad((lane >> 5) ? m1 : m0, L, 0.f, 0.f, tag));
      set_flag(FL_OPUB);
      STAMP(7);
    }
  } else if (wave < 10) {
    __builtin_amdgcn_s_setprio(1);
    const int w2 = wave & 1;
    const int r = j >> 2, c4 = j & 3;
    const rsrc_t rw = mk(a.wo, NQ * D * 2);
    const rsrc_t rws = mk(a.ws, WS_END);
    u32 vo[9];
#pragma unroll
    for (int i = 0; i < 9; ++i) {
      const u32 id = (w2 + 2 * i) * 64 + lane;
      const u32 row = (id * 3641u) >> 16, c = id - row * 18;
      vo[i] = (64 * r + row) * 1152 + c4 * 288 + c * 16;
      asm volatile("" : "+v"(vo[i]));
    }
    const Tag tag = gettag();
    wait_flag(FL_G0, 3);
    STAMP(1);
#pragma unroll
    for (int i = 0; i < 9; ++i) dma16(rw, vo[i], 0, L_WO + (w2 + 2 * i) * 1024);
    __builtin_amdgcn_s_setprio(3);
    STAMP(2);
    VMCNT(0);
    add_flag(FL_WOL);
    wait_flag(FL_WOL, 2);
    STAMP(3);
    const u32 lr = lane >> 4, q4 = (lane & 15) >> 2, p4 = lane & 3;
    const u32 badr = L_WO + (8 * lr + q4) * 288 + p4 * 8 + w2 * 32;
    s16x4 bb[5][4];
#pragma unroll
    for (int t = 0; t < 5; ++t)
#pragma unroll
      for (int u = 0; u < 4; ++u)
        bb[t][u] = __builtin_amdgcn_ds_read_tr16_b64_v4i16((LAS s16x4*)(smem + badr + ((u >> 1) * 32 + (u & 1) * 4) * 288 + ((t == 4 && w2) ? 3 : t) * 64));
    u32 offA, offB;
    {
      const u32 cqA = c4 * 36 + (w2 + 2 * lr) * 4 + q4, cqB = c4 * 36 + 32 + q4;
      offA = p4 == 0 ? WS_FIN + (((cqA >> 1) * 2) * 64 + r) * 32 + (cqA & 1) * 16 : OOB;
      offB = (w2 == 0 && lr == 0 && p4 == 0) ? WS_FIN + (((cqB >> 1) * 2) * 64 + r) * 32 + (cqB & 1) * 16 : OOB;
      asm volatile("" : "+v"(offA), "+v"(offB));
    }
#pragma unroll
    for (int t = 0; t < 5; ++t)
#pragma unroll
      for (int u = 0; u < 4; ++u) asm volatile("" : "+v"(bb[t][u]));
    wait_flag_tight(FL_O, 1);
    STAMP(4);
    const s16x8 a0 = *(const LAS s16x8*)(smem + L_OI + lr * 16), a1 = *(const LAS s16x8*)(smem + L_OI + 64 + lr * 16);
    f32x4 acc[5];
#pragma unroll
    for (int t = 0; t < 5; ++t) {
      f32x4 c = {0.f, 0.f, 0.f, 0.f};
      c = __builtin_amdgcn_mfma_f32_16x16x32_bf16(__builtin_bit_cast(bf16x8, a0), __builtin_bit_cast(bf16x8, __builtin_shufflevector(bb[t][0], bb[t][1], 0, 1, 2, 3, 4, 5, 6, 7)), c, 0, 0, 0);
      c = __builtin_amdgcn_mfma_f32_16x16x32_bf16(__builtin_bit_cast(bf16x8, a1), __builtin_bit_cast(bf16x8, __builtin_shufflevector(bb[t][2], bb[t][3], 0, 1, 2, 3, 4, 5, 6, 7)), c, 0, 0, 0);
      acc[t] = c;
    }
    st_quad(rws, offA, quad_of_lanes(sel4(lr, acc[0][0], acc[1][0], acc[2][0], acc[3][0]), tag));
    if (w2 == 0) st_quad(rws, offB, quad_of_lanes(acc[4][0], tag));
    add_flag(FL_FIN);
    STAMP(5);
  } else if (wave == 10) {
    __builtin_amdgcn_s_setprio(1);
    const int r = j >> 2, c4 = j & 3;
    const rsrc_t rw = mk(a.wd, F * D * 2);
    const rsrc_t rws = mk(a.ws, WS_END);
    u32 vo[21];
#pragma unroll
    for (int i = 0; i < 21; ++i) {
      const u32 id = i * 64 + lane;
      const u32 row = (id * 3641u) >> 16, c = id - row * 18;
      const u32 grow = row >= 64 ? 4096 + 8 * r + (row - 64) : 64 * r + row;
      vo[i] = id < 1296u ? grow * 1152 + c4 * 288 + c * 16 : OOB;
      asm volatile("" : "+v"(vo[i]));
    }
    const Tag tag = gettag();
    wait_flag(FL_G0, 3);
    wait_flag(FL_MX, 4);
    STAMP(1);
#pragma unroll
    for (int i = 0; i < 21; ++i) dma16(rw, vo[i], 0, L_K + i * 1024);
    __builtin_amdgcn_s_setprio(3);
    STAMP(2);
    VMCNT(DH_LEFT);
    *flg(FL_DH) = 1u;
    VMCNT(0);
    set_flag(FL_DL);
    STAMP(3);
    down_gemv(0, j, lane, tag, rws);
    STAMP(5);
  } else {
    __builtin_amdgcn_s_setprio(3);
    const rsrc_t rws = mk(a.ws, WS_END);
    const int h = j >> 3, hf = (j >> 2) & 1;
    const Tag tag = gettag();
    const u32 lr = lane >> 4;
    {
      const u32 base = WS_Q + (j & ~15) * 256 + lane * 16;
      wait_flag(FL_QPUB, 1);
      __builtin_amdgcn_s_sleep(Q_DELAY);
      wait_flag(FL_VH, 1);
      STAMP(1);
      v4u g[4];
      GATHER(g, 4, base + i * 1024);
      f32x4 v[4];
#pragma unroll
      for (int i = 0; i < 4; ++i) v[i] = gv4(g[i]);
      float qv[4];
#pragma unroll
      for (int k = 0; k < 4; ++k) qv[k] = swap16_add(swap32_add(v[0][k], v[2][k]), swap32_add(v[1][k], v[3][k]));
      *(LAS v2u*)(smem + L_QI + (lr * 64 + (lane & 15) * 4) * 2) = v2u{pk(qv[0], qv[1]), pk(qv[2], qv[3])};
      set_flag(FL_Q);
      STAMP(2);
    }
    {
      const u32 bo = WS_OP + ((h * 16 + lr) * 32 + hf * 16 + (lane & 15)) * 16;
      const u32 bl = WS_ML + (h * 16 + ((lane & 12) | ((lane + lr) & 3))) * 16;
      wait_flag(FL_OPUB, 1);
      __builtin_amdgcn_s_sleep(O_DELAY);
      wait_flag(FL_DH, 1);
      STAMP(5);
      v4u g[5];
      GATHER(g, 5, (i == 0 ? bl : bo + (i - 1) * 2048));
      STAMP(6);
      const float m = gv(g[0].x), l = gv(g[0].y);
      float M = m; M = fmaxf(M, dpp<DPP_XOR1>(M)); M = fmaxf(M, dpp<DPP_XOR2>(M)); M = fmaxf(M, dpp<DPP_HMIRROR>(M)); M = fmaxf(M, dpp<DPP_MIRROR>(M));
      const float wt = __builtin_amdgcn_exp2f(m - M);
      float L = wt * l; L += dpp<DPP_XOR1>(L); L += dpp<DPP_XOR2>(L); L += dpp<DPP_HMIRROR>(L); L += dpp<DPP_MIRROR>(L);
      f32x4 acc = dpp<0x150>(wt) * gv4(g[1]);
      acc += dpp<0x154>(wt) * gv4(g[2]);
      acc += dpp<0x158>(wt) * gv4(g[3]);
      acc += dpp<0x15C>(wt) * gv4(g[4]);
      const float o = swap16_add(swap32_add(acc[0], acc[2]), swap32_add(acc[1], acc[3])) * __builtin_amdgcn_rcpf(L);
      ((LAS bf16_t*)(smem + L_OI))[(lane & 15) * 4 + lr] = (bf16_t)(pk(o, o) & 0xFFFFu);
      set_flag(FL_O);
      STAMP(7);
    }
  }
  STAMP(9);
  STAMP_FLUSH();
}

}

extern "C" void kernel_launch(void* const* d_in, const int* in_sizes, int n_in, void* d_out, int out_size, void* d_ws, size_t ws_size,
                              hipStream_t stream) {
  Args a;
  a.x = (const bf16_t*)d_in[0]; a.kc = (const bf16_t*)d_in[1]; a.vc = (const bf16_t*)d_in[2];
  a.wq = (const bf16_t*)d_in[3]; a.wo = (const bf16_t*)d_in[4];
  a.wg = (const bf16_t*)d_in[6]; a.wu = (const bf16_t*)d_in[7]; a.wd = (const bf16_t*)d_in[8];
  a.out = (bf16_t*)d_out; a.ws = (unsigned char*)d_ws;
  (void)hipFuncSetAttribute((const void*)fused_kernel, hipFuncAttributeMaxDynamicSharedMemorySize, L_END);
  fused_kernel<<<256, NTHR, L_END, stream>>>(a);
}
```

```cpp
#define LEAK 1.5e-4f
#include <hip/hip_runtime.h>
#include <stdint.h>

typedef unsigned short bf16_t;
typedef unsigned u32;
typedef unsigned long long u64;
typedef u32 v4u __attribute__((ext_vector_type(4)));
typedef u32 v2u __attribute__((ext_vector_type(2)));
typedef float f32x4 __attribute__((ext_vector_type(4)));
typedef short s16x4 __attribute__((ext_vector_type(4)));
typedef short s16x8 __attribute__((ext_vector_type(8)));
typedef __bf16 bf16x2 __attribute__((ext_vector_type(2)));
typedef __bf16 bf16x8 __attribute__((ext_vector_type(8)));
#define LAS __attribute__((address_space(3)))
#define GAS __attribute__((address_space(1)))
typedef __amdgpu_buffer_rsrc_t rsrc_t;

namespace {

constexpr int D = 576, SK = 65536, DH = 128, WIN = 4096, F = 4608, NQ = 4096;
constexpr int NWAVES = 12, NTHR = NWAVES * 64;

constexpr u32 WS_EPOCH = 0;
constexpr u32 WS_Q = 4096;
constexpr u32 WS_GM = WS_Q + 256 * 256;
constexpr u32 WS_GP = WS_GM + 64 * 8 * 256;
constexpr u32 WS_OP = WS_GP + 256 * 256;
constexpr u32 WS_ML = WS_OP + 32 * 16 * 512;
constexpr u32 WS_FIN = WS_ML + 32 * 16 * 16;
constexpr u32 WS_END = WS_FIN + 72 * 2 * 64 * 32;
constexpr u32 WS_STAMP = 4u << 20;

constexpr u32 L_K = 0;
constexpr u32 L_V = 65536;
constexpr u32 L_WO = 131072;
constexpr u32 L_XN = L_WO + 18432;
constexpr u32 L_QI = L_XN + 2304;
constexpr u32 L_PI = L_QI + 512;
constexpr u32 L_OW = L_PI + 1024;
constexpr u32 L_HI = L_OW + 4096;
constexpr u32 L_OI = L_HI + 160;
constexpr u32 L_WT = L_OI + 128;
constexpr u32 L_MX = L_WT + 64;
constexpr u32 L_TAG = L_MX + 64;
constexpr u32 L_FLG = L_TAG + 32;
constexpr u32 L_END = L_FLG + 128;
static_assert(L_END <= 163840, "LDS");
enum { FL_Q, FL_VISS, FL_MX, FL_OW, FL_OPUB, FL_H, FL_O, FL_WOL, FL_LS, FL_FIN, FL_G0, FL_TAG, FL_W0, FL_M2, FL_QPUB, FL_HPUB, FL_DL, FL_VH, FL_DH };

#define LDPOL 2
#define DMAPOL "nt"
constexpr int AUX_NT = LDPOL, AUX_SC1 = 16;
constexpr u32 SPIN_CAP = 1u << 22;
constexpr u32 OOB = 0x7FFFFFF0u;
#define KVROT 1
#define KVTILE(t, s) ((t) * 16 + (((s) + KVROT * (t)) & 15))
#define Q_DELAY 4
#define H_DELAY 8
#define ML_DELAY 12
#define O_DELAY 8
#define F_DELAY 10
#define DH_LEFT 10
#define VH_AT 10

extern __shared__ __attribute__((aligned(16))) unsigned char smem[];

__device__ __forceinline__ float lo2f(u32 p) { return __uint_as_float(p << 16); }
__device__ __forceinline__ float hi2f(u32 p) { return __uint_as_float(p & 0xFFFF0000u); }
__device__ __forceinline__ u32 pk(float a, float b) {
  bf16x2 r = {(__bf16)a, (__bf16)b};
  return __builtin_bit_cast(u32, r);
}
__device__ __forceinline__ float rbf(float f) { return lo2f(pk(f, f)); }
__device__ __forceinline__ float sel(bool c, float a, float b) { return c ? a : b; }
__device__ __forceinline__ float sel4(u32 g, float a, float b, float c, float d) {
  const bool b0 = g & 1, b1 = g & 2;
  const float lo = b0 ? b : a, hi = b0 ? d : c;
  return b1 ? hi : lo;
}
__device__ __forceinline__ rsrc_t mk(const void* p, u32 bytes) {
  return __builtin_amdgcn_make_buffer_rsrc((void*)p, 0, bytes, 0x00020000);
}
__device__ __forceinline__ float dot2(u32 w, u32 x, float acc) {
  return __builtin_amdgcn_fdot2_f32_bf16(__builtin_bit_cast(bf16x2, w), __builtin_bit_cast(bf16x2, x), acc, false);
}
template <int CTRL> __device__ __forceinline__ float dpp(float x) {
  return __builtin_bit_cast(float, __builtin_amdgcn_mov_dpp(__builtin_bit_cast(int, x), CTRL, 0xf, 0xf, true));
}
constexpr int DPP_XOR1 = 0xB1, DPP_XOR2 = 0x4E, DPP_HMIRROR = 0x141, DPP_MIRROR = 0x140, DPP_ROR4 = 0x124, DPP_ROR8 = 0x128;
__device__ __forceinline__ float swap16_add(float a, float b) {
  auto s = __builtin_amdgcn_permlane16_swap(__float_as_uint(a), __float_as_uint(b), false, false);
  return __uint_as_float(s[0]) + __uint_as_float(s[1]);
}
__device__ __forceinline__ float swap32_add(float a, float b) {
  auto s = __builtin_amdgcn_permlane32_swap(__float_as_uint(a), __float_as_uint(b), false, false);
  return __uint_as_float(s[0]) + __uint_as_float(s[1]);
}
__device__ __forceinline__ float swap16_max(float a) {
  auto s = __builtin_amdgcn_permlane16_swap(__float_as_uint(a), __float_as_uint(a), false, false);
  return fmaxf(__uint_as_float(s[0]), __uint_as_float(s[1]));
}
__device__ __forceinline__ float swap32_max(float a) {
  auto s = __builtin_amdgcn_permlane32_swap(__float_as_uint(a), __float_as_uint(a), false, false);
  return fmaxf(__uint_as_float(s[0]), __uint_as_float(s[1]));
}
__device__ __forceinline__ float wave_sum(float x) {
  x += dpp<DPP_XOR1>(x); x += dpp<DPP_XOR2>(x); x += dpp<DPP_HMIRROR>(x); x += dpp<DPP_MIRROR>(x);
  x = swap16_add(x, x); x = swap32_add(x, x);
  return x;
}
__device__ __forceinline__ float wave_max(float x) {
  x = fmaxf(x, dpp<DPP_XOR1>(x)); x = fmaxf(x, dpp<DPP_XOR2>(x)); x = fmaxf(x, dpp<DPP_HMIRROR>(x)); x = fmaxf(x, dpp<DPP_MIRROR>(x));
  x = swap16_max(x); x = swap32_max(x);
  return x;
}

struct Tag { u32 t0, t1, t2, t3; };
__device__ __forceinline__ u32 q22(float v, u32 t) { return ((__float_as_uint(v) + 0x200u) & 0xFFFFFC00u) | t; }
__device__ __forceinline__ float r22(float v) { return __uint_as_float((__float_as_uint(v) + 0x200u) & 0xFFFFFC00u); }
__device__ __forceinline__ v4u quad(float a, float b, float c, float d, Tag t) { return v4u{q22(a, t.t0), q22(b, t.t1), q22(c, t.t2), q22(d, t.t3)}; }
__device__ __forceinline__ float gv(u32 d) { return __uint_as_float(d & 0xFFFFFC00u); }
__device__ __forceinline__ f32x4 gv4(v4u v) { return f32x4{gv(v.x), gv(v.y), gv(v.z), gv(v.w)}; }
struct Chk {
  u32 a = 0;
  __device__ __forceinline__ void add(v4u v, Tag t) {
    a = __builtin_amdgcn_bitop3_b32(v.x, t.t0, a, 0xBE); a = __builtin_amdgcn_bitop3_b32(v.y, t.t1, a, 0xBE);
    a = __builtin_amdgcn_bitop3_b32(v.z, t.t2, a, 0xBE); a = __builtin_amdgcn_bitop3_b32(v.w, t.t3, a, 0xBE);
  }
  __device__ __forceinline__ bool ok() const { return __all((a & 0x3FFu) == 0u); }
};
__device__ __forceinline__ void st_quad(rsrc_t rs, u32 off, v4u q) { __builtin_amdgcn_raw_buffer_store_b128(q, rs, off, 0, AUX_SC1); }
__device__ __forceinline__ v4u ld_quad(rsrc_t rs, u32 off) { return __builtin_amdgcn_raw_buffer_load_b128(rs, off, 0, AUX_SC1); }
#define GATHER(g, N, OFFEXPR)                                              \
  _Pragma("unroll 1") for (u32 sp_ = 0; sp_ < SPIN_CAP; ++sp_) {          \
    asm volatile("" ::: "memory");                                         \
    Chk c_;                                                                \
    _Pragma("unroll") for (int i = 0; i < N; ++i) g[i] = ld_quad(rws, OFFEXPR); \
    _Pragma("unroll") for (int i = 0; i < N; ++i) c_.add(g[i], tag);      \
    COUNT_ATTEMPT();                                                       \
    if (c_.ok()) break;                                                    \
  }

__device__ __forceinline__ volatile LAS u32* flg(int i) { return (volatile LAS u32*)(smem + L_FLG) + i; }
__device__ __forceinline__ void wait_flag(int i, u32 want) {
  volatile LAS u32* p = flg(i);
#pragma unroll 1
  for (u32 s = 0; s < SPIN_CAP; ++s) {
    if (__builtin_amdgcn_readfirstlane(*p) >= want) break;
    __builtin_amdgcn_s_sleep(1);
  }
  asm volatile("" ::: "memory");
}
__device__ __forceinline__ void wait_flag_tight(int i, u32 want) {
  volatile LAS u32* p = flg(i);
#pragma unroll 1
  for (u32 s = 0; s < SPIN_CAP; ++s) {
    if (__builtin_amdgcn_readfirstlane(*p) >= want) break;
  }
  asm volatile("" ::: "memory");
}
__device__ __forceinline__ Tag gettag() {
  wait_flag(FL_TAG, 1);
  const v4u tg = *(const LAS v4u*)(smem + L_TAG);
  Tag t{(u32)__builtin_amdgcn_readfirstlane(tg.x), (u32)__builtin_amdgcn_readfirstlane(tg.y), (u32)__builtin_amdgcn_readfirstlane(tg.z), (u32)__builtin_amdgcn_readfirstlane(tg.w)};
  asm volatile("" : "+s"(t.t0), "+s"(t.t1), "+s"(t.t2), "+s"(t.t3));
  return t;
}
__device__ __forceinline__ void set_flag(int i) {
  asm volatile("s_waitcnt lgkmcnt(0)" ::: "memory");
  *flg(i) = 1u;
  asm volatile("" ::: "memory");
}
__device__ __forceinline__ void add_flag(int i) {
  asm volatile("s_waitcnt lgkmcnt(0)" ::: "memory");
  if ((threadIdx.x & 63) == 0) __hip_atomic_fetch_add((LAS u32*)(smem + L_FLG) + i, 1u, __ATOMIC_RELAXED, __HIP_MEMORY_SCOPE_WORKGROUP);
  asm volatile("" ::: "memory");
}
__device__ __forceinline__ u32 bump_flag(int i) {
  asm volatile("s_waitcnt lgkmcnt(0)" ::: "memory");
  u32 r = 0;
  if ((threadIdx.x & 63) == 0) r = __hip_atomic_fetch_add((LAS u32*)(smem + L_FLG) + i, 1u, __ATOMIC_RELAXED, __HIP_MEMORY_SCOPE_WORKGROUP);
  r = __builtin_amdgcn_readfirstlane(r);
  asm volatile("" ::: "memory");
  return r;
}
__device__ __forceinline__ void dma16(rsrc_t rs, u32 voff, u32 soff, u32 ldsoff) {
  asm volatile("s_mov_b32 m0, %0\n\ts_nop 0\n\tbuffer_load_dwordx4 %1, %2, %3 offen " DMAPOL " lds" ::"s"(ldsoff), "v"(voff), "s"(rs), "s"(soff) : "m0", "memory");
}
#define COUNT_ATTEMPT() do { } while (0)
#define VMCNT_(n) asm volatile("s_waitcnt vmcnt(" #n ")" ::: "memory")
#define VMCNT(n) VMCNT_(n)
#define SB() __builtin_amdgcn_sched_barrier(0)
#define BARRIER() do { __builtin_amdgcn_sched_barrier(0); asm volatile("s_waitcnt lgkmcnt(0)" ::: "memory"); __builtin_amdgcn_s_barrier(); asm volatile("" ::: "memory"); __builtin_amdgcn_sched_barrier(0); } while (0)

#define STAMP(i) do { } while (0)
#define STAMP_FLUSH() do { } while (0)

struct Args {
  const bf16_t* x; const bf16_t* kc; const bf16_t* vc; const bf16_t* wq; const bf16_t* wo;
  const bf16_t* wg; const bf16_t* wu; const bf16_t* wd; bf16_t* out; unsigned char* ws;
};

__device__ __forceinline__ float silu_b(float v) {
  return rbf(v * __builtin_amdgcn_rcpf(1.f + __builtin_amdgcn_exp2f(-1.4426950408889634f * v)));
}
__device__ __forceinline__ v4u quad_of_lanes(float v, Tag tag) {
  return quad(dpp<0x00>(v), dpp<0x55>(v), dpp<0xAA>(v), dpp<0xFF>(v), tag);
}

__device__ __forceinline__ void down_gemv(int part, int j, int lane, Tag tag, rsrc_t rws) {
  const int r = j >> 2, c4 = j & 3;
  const u32 lr = lane >> 4, q4 = (lane & 15) >> 2, p4 = lane & 3;
  const u32 badr = L_K + (8 * lr + q4) * 288 + p4 * 8 + part * 96;
  const u32 row4 = 64 + 4 * lr + q4;
  const u32 badr4 = L_K + (row4 < 72 ? row4 : 71) * 288 + p4 * 8 + part * 96;
  const u32 cq = c4 * 36 + (3 * part + lr) * 4 + q4;
  u32 off = (lr < 3 && p4 == 0) ? WS_FIN + (((cq >> 1) * 2 + 1) * 64 + r) * 32 + (cq & 1) * 16 : OOB;
  asm volatile("" : "+v"(off));
  wait_flag(FL_DL, 1);
  s16x4 b[3][5];
#pragma unroll
  for (int t = 0; t < 3; ++t) {
#pragma unroll
    for (int u = 0; u < 4; ++u) b[t][u] = __builtin_amdgcn_ds_read_tr16_b64_v4i16((LAS s16x4*)(smem + badr + ((u >> 1) * 32 + (u & 1) * 4) * 288 + t * 32));
    b[t][4] = __builtin_amdgcn_ds_read_tr16_b64_v4i16((LAS s16x4*)(smem + badr4 + t * 32));
  }
#pragma unroll
  for (int t = 0; t < 3; ++t)
#pragma unroll
    for (int u = 0; u < 5; ++u) asm volatile("" : "+v"(b[t][u]));
  wait_flag_tight(FL_H, 1);
  const s16x8 a0 = *(const LAS s16x8*)(smem + L_HI + lr * 16), a1 = *(const LAS s16x8*)(smem + L_HI + 64 + lr * 16);
  const s16x4 a2 = *(const LAS s16x4*)(smem + L_HI + 128 + lr * 8);
  f32x4 acc[3];
#pragma unroll
  for (int t = 0; t < 3; ++t) {
    f32x4 c = {0.f, 0.f, 0.f, 0.f};
    c = __builtin_amdgcn_mfma_f32_16x16x16bf16_1k(a2, b[t][4], c, 0, 0, 0);
    c = __builtin_amdgcn_mfma_f32_16x16x32_bf16(__builtin_bit_cast(bf16x8, a0), __builtin_bit_cast(bf16x8, __builtin_shufflevector(b[t][0], b[t][1], 0, 1, 2, 3, 4, 5, 6, 7)), c, 0, 0, 0);
    c = __builtin_amdgcn_mfma_f32_16x16x32_bf16(__builtin_bit_cast(bf16x8, a1), __builtin_bit_cast(bf16x8, __builtin_shufflevector(b[t][2], b[t][3], 0, 1, 2, 3, 4, 5, 6, 7)), c, 0, 0, 0);
    acc[t] = c;
  }
  st_quad(rws, off, quad_of_lanes(sel4(lr, acc[0][0], acc[1][0], acc[2][0], acc[2][0]), tag));
  add_flag(FL_FIN);
}

__global__ void __launch_bounds__(NTHR, 3) fused_kernel(Args a) {
  const int tid = threadIdx.x, lane = tid & 63;
  const int wave = __builtin_amdgcn_readfirstlane(tid >> 6);
  const int b = blockIdx.x;
  const int j = (b & 7) * 32 + (b >> 3);
  STAMP(0);
  u64 leak_t0; asm volatile("s_memrealtime %0\n\ts_waitcnt lgkmcnt(0)" : "=s"(leak_t0)::"memory");
  if (wave == 11) {
    if (lane < 32) *flg(lane) = 0u;
    if (lane >= 32 && lane < 36) *(LAS u32*)(smem + L_HI + 144 + (lane - 32) * 4) = 0u;
  }
  BARRIER();

  if (__builtin_expect(wave < 4, 1)) {
    __builtin_amdgcn_s_setprio(3);
    const int kp = j & 3, r = j >> 2;
    const bf16_t* wm; u32 row0, stride, strip, nrows, pub;
    if (wave == 0) { wm = a.wq; row0 = 144 * kp; stride = NQ * 2; strip = r; nrows = 144; pub = WS_Q + j * 256; }
    else if (wave < 3) {
      const int quarter = 2 * (kp & 1) + (wave - 1);
      wm = (kp >> 1) ? a.wu : a.wg; row0 = 144 * quarter; stride = F * 2; nrows = 144;
      strip = (r & ~7) | ((r + 3 * wave) & 7);
      pub = WS_GM + ((strip * 2 + (kp >> 1)) * 4 + quarter) * 256;
    } else { wm = ((j >> 4) & 1) ? a.wu : a.wg; row0 = 36 * (j & 15); stride = F * 2; strip = 64 + (j >> 5); nrows = 36; pub = WS_GP + j * 256; }
    const rsrc_t rw = mk((const unsigned char*)wm + (size_t)row0 * stride + strip * 128, (nrows - 1) * stride + 128);
    const rsrc_t rx = mk(a.x, D * 2);
    const rsrc_t rws = mk(a.ws, WS_END);
    const u64 ep = __hip_atomic_load((GAS u64*)(a.ws + WS_EPOCH), __ATOMIC_RELAXED, __HIP_MEMORY_SCOPE_AGENT);
    u32 xr[5];
#pragma unroll
    for (int i = 0; i < 5; ++i) xr[i] = __builtin_amdgcn_raw_buffer_load_b32(rx, lane * 4, i * 256, 0);
    const u32 rs8 = lane >> 3;
    const u32 voff = rs8 * stride + (lane & 7) * 16;
    u64 tg = ep;
    Tag tag;
    auto layernorm = [&](v4u (&av)[18]) {
#pragma unroll
      for (int i = 0; i < 5; ++i) asm volatile("" : "+v"(xr[i]));
      asm volatile("" : "+v"(tg));
      tg += 1;
      {
        const u64 T = ((u64)(u32)__builtin_amdgcn_readfirstlane((u32)(tg >> 32)) << 32) | (u32)__builtin_amdgcn_readfirstlane((u32)tg);
        const u64 H = T * 0x9E3779B97F4A7C15ull;
        tag = Tag{(u32)T & 0x3FFu, (u32)(H >> 54), (u32)(H >> 44) & 0x3FFu, (u32)(H >> 34) & 0x3FFu};
        if (wave == 0) {
          if (lane == 0) { *(LAS v4u*)(smem + L_TAG) = v4u{tag.t0, tag.t1, tag.t2, tag.t3}; *(LAS v2u*)(smem + L_TAG + 16) = v2u{(u32)T, (u32)(T >> 32)}; }
          set_flag(FL_TAG);
        }
      }
      float s = 0.f, q = 0.f;
#pragma unroll
      for (int i = 0; i < 5; ++i) { s = dot2(xr[i], 0x3F803F80u, s); q = dot2(xr[i], xr[i], q); }
      s = wave_sum(s); q = wave_sum(q);
      const float mean = s * (1.f / 576.f);
      const float var = rbf(fmaxf(fmaf(-mean, mean, q * (1.f / 576.f)), 0.f));
      const float mb = rbf(mean);
      const float inv = __builtin_amdgcn_rcpf(rbf(__builtin_amdgcn_sqrtf(rbf(var + 1e-5f))));
      LAS v2u* xn = (LAS v2u*)(smem + L_XN);
#pragma unroll
      for (int i = 0; i < 5; ++i) {
        const u32 d = pk(lo2f(xr[i]) - mb, hi2f(xr[i]) - mb);
        const u32 n = pk(lo2f(d) * inv, hi2f(d) * inv);
        if (i < 4 || lane < 32) xn[i * 64 + lane] = v2u{__builtin_amdgcn_perm(n, n, 0x01000100u), __builtin_amdgcn_perm(n, n, 0x03020302u)};
      }
      asm volatile("s_waitcnt lgkmcnt(0)" ::: "memory");
      u32 xp[18];
      const LAS u32* xs = (const LAS u32*)(smem + L_XN) + row0 + rs8;
#pragma unroll
      for (int i = 0; i < 18; ++i) xp[i] = xs[(wave == 3 && i > 4) ? 0 : 8 * i];
      if (wave == 3 && rs8 >= 4) xp[4] = 0u;
      u32 msk[4];
#pragma unroll
      for (int d = 0; d < 4; ++d) msk[d] = (((lane & 7) >> 1) == d) ? ((lane & 1) ? 0xFFFF0000u : 0x0000FFFFu) : 0u;
#pragma unroll
      for (int i = 0; i < 18; ++i) av[i] = v4u{xp[i] & msk[0], xp[i] & msk[1], xp[i] & msk[2], xp[i] & msk[3]};
    };
    auto gemv_publish = [&](const v4u (&av)[18], const v4u (&w)[18]) {
      f32x4 da = {0.f, 0.f, 0.f, 0.f}, db = {0.f, 0.f, 0.f, 0.f};
#pragma unroll
      for (int i = 0; i < 18; ++i) {
        if (i & 1) db = __builtin_amdgcn_mfma_f32_16x16x32_bf16(__builtin_bit_cast(bf16x8, av[i]), __builtin_bit_cast(bf16x8, w[i]), db, 0, 0, 0);
        else da = __builtin_amdgcn_mfma_f32_16x16x32_bf16(__builtin_bit_cast(bf16x8, av[i]), __builtin_bit_cast(bf16x8, w[i]), da, 0, 0, 0);
      }
      da += db;
      f32x4 res;
#pragma unroll
      for (int k = 0; k < 4; ++k) {
        const float up = dpp<DPP_ROR8>(da[k]);
        res[k] = da[k] + __uint_as_float(__builtin_amdgcn_permlane32_swap(__float_as_uint(up), __float_as_uint(up), false, false)[1]);
      }
      const u32 off = ((lane & 0x28) == 0) ? pub + (2 * (lane & 7) + (lane >> 4)) * 16 : OOB;
      st_quad(rws, off, quad(res[0], res[1], res[2], res[3], tag));
    };
    if (__builtin_expect(wave == 0, 1)) {
      v4u w[18], av[18];
#pragma unroll
      for (int i = 0; i < 18; ++i) w[i] = __builtin_amdgcn_raw_buffer_load_b128(rw, voff, 8 * i * stride, AUX_NT);
      SB();
      set_flag(FL_W0);
      STAMP(1);
      layernorm(av);
      STAMP(2);
      gemv_publish(av, w);
      set_flag(FL_QPUB);
      STAMP(3);
      {
        const u32 bm = WS_GM + r * 2048 + lane * 16;
        const u32 bp = WS_GP + ((r >> 3) * 32 + (lane >> 1)) * 256 + ((r & 7) * 2 + (lane & 1)) * 16;
        wait_flag(FL_HPUB, 3);
        __builtin_amdgcn_s_sleep(H_DELAY);
        wait_flag(FL_DH, 1);
        STAMP(4);
        v4u g[3];
        GATHER(g, 3, (i < 2 ? bm + i * 1024 : bp));
        STAMP(5);
        const f32x4 gg = gv4(g[0]), uu = gv4(g[1]), tt = gv4(g[2]);
        const bool tail = lane >= 16;
        float hv[4];
#pragma unroll
        for (int k = 0; k < 4; ++k) {
          float m = swap16_add(gg[k], uu[k]);
          m = swap32_add(m, m);
          float t = tt[k];
          t += dpp<DPP_XOR2>(t); t += dpp<DPP_ROR4>(t); t += dpp<DPP_ROR8>(t); t = swap16_add(t, t);
          const auto sm = __builtin_amdgcn_permlane16_swap(__float_as_uint(m), __float_as_uint(m), false, false);
          const auto stl = __builtin_amdgcn_permlane32_swap(__float_as_uint(t), __float_as_uint(t), false, false);
          const float gs = __uint_as_float(tail ? stl[0] : sm[0]), us = __uint_as_float(tail ? stl[1] : sm[1]);
          hv[k] = silu_b(rbf(gs)) * rbf(us);
        }
        if (lane < 18) *(LAS v2u*)(smem + L_HI + lane * 8) = v2u{pk(hv[0], hv[1]), pk(hv[2], hv[3])};
        set_flag(FL_H);
        STAMP(6);
      }
      if ((j & 31) < 9) {
        const int f = (j >> 5) * 9 + (j & 31);
        const u32 mycol = f * 8 + 4 * (lane & 1) + 2 * ((lane >> 4) & 1) + ((lane >> 3) & 1);
        const u32 xo = __builtin_amdgcn_raw_buffer_load_b16(rx, mycol * 2, 0, 0);
        const u32 base = WS_FIN + f * 4096 + lane * 16;
        wait_flag(FL_FIN, 5);
        __builtin_amdgcn_s_sleep(F_DELAY);
        STAMP(7);
        v4u g[4];
        GATHER(g, 4, base + i * 1024);
        STAMP(1);
        const f32x4 sv = gv4(g[0]) + gv4(g[1]), fv = gv4(g[2]) + gv4(g[3]);
        float a4[4];
#pragma unroll
        for (int k = 0; k < 4; ++k) a4[k] = swap32_add(sv[k], fv[k]);
        const float a0 = swap16_add(a4[0], a4[2]), a1 = swap16_add(a4[1], a4[3]);
        float b0 = a0 + dpp<DPP_XOR2>(a0), b1 = a1 + dpp<DPP_XOR2>(a1);
        b0 += dpp<DPP_ROR4>(b0); b1 += dpp<DPP_ROR4>(b1);
        b0 += dpp<DPP_ROR8>(b0); b1 += dpp<DPP_ROR8>(b1);
        const float t = (lane & 8) ? b1 : b0;
        const auto sf = __builtin_amdgcn_permlane32_swap(__float_as_uint(t), __float_as_uint(t), false, false);
        const float sb = rbf(__uint_as_float(sf[0])), fb = rbf(__uint_as_float(sf[1]));
        float o = lo2f(xo) + rbf(sb * silu_b(fb));
        if (f == 0) { u64 t1; asm volatile("s_memrealtime %0\n\ts_waitcnt lgkmcnt(0)" : "=s"(t1)::"memory"); o += (float)(u32)(t1 - leak_t0) * (LEAK * 1e-2f); }
        if ((lane & 0x26) == 0) *(GAS bf16_t*)((GAS unsigned char*)a.out + mycol * 2) = (bf16_t)(pk(o, o) & 0xFFFFu);
        if (f == 0 && lane == 0) { const v2u nx = *(const LAS v2u*)(smem + L_TAG + 16); __hip_atomic_store((GAS u64*)(a.ws + WS_EPOCH), (u64)nx.x | ((u64)nx.y << 32), __ATOMIC_RELAXED, __HIP_MEMORY_SCOPE_AGENT); }
        STAMP(2);
      }
    } else {
      v4u w[18], av[18];
      layernorm(av);
      STAMP(2);
      __builtin_amdgcn_s_setprio(1);
#pragma unroll
      for (int i = 0; i < 18; ++i) asm volatile("" : "+v"(av[i]));
      wait_flag(FL_VISS, 4);
#pragma unroll
      for (int i = 0; i < 18; ++i) w[i] = __builtin_amdgcn_raw_buffer_load_b128(rw, voff, 8 * i * stride, AUX_NT);
      SB();
      add_flag(FL_G0);
      __builtin_amdgcn_s_setprio(3);
      STAMP(5);
      gemv_publish(av, w);
      add_flag(FL_HPUB);
      STAMP(3);
      if (wave >= 2) {
        down_gemv(wave - 1, j, lane, tag, rws);
        STAMP(6);
      }
    }
  } else if (__builtin_expect(wave < 8, 1)) {
    __builtin_amdgcn_s_setprio(0);
    const int w = wave - 4, g = j >> 4, s = j & 15;
    const rsrc_t rk = mk(a.kc + ((size_t)g * SK + (SK - WIN)) * DH, WIN * DH * 2);
    const rsrc_t rv = mk(a.vc + ((size_t)g * SK + (SK - WIN)) * DH, WIN * DH * 2);
    const rsrc_t rws = mk(a.ws, WS_END);
    const u32 lr = lane >> 4;
    u32 vo[4];
#pragma unroll
    for (int sub = 0; sub < 4; ++sub) { vo[sub] = sub * 1024 + lr * 256 + ((((lane & 15) ^ (lr << 2)) ^ sub) << 4); asm volatile("" : "+v"(vo[sub])); }
    wait_flag(FL_W0, 1);
#pragma unroll
    for (int p = 0; p < 16; ++p) dma16(rk, vo[p & 3], KVTILE(4 * w + (p >> 2), s) * 4096, L_K + w * 16384 + p * 1024);
#pragma unroll
    for (int p = 0; p < 16; ++p) {
      dma16(rv, vo[p & 3], KVTILE(4 * w + (p >> 2), s) * 4096, L_V + w * 16384 + p * 1024);
      if (p == VH_AT && w == 0) *flg(FL_VH) = 1u;
    }
    add_flag(FL_VISS);
    __builtin_amdgcn_s_setprio(3);
    STAMP(1);
    const Tag tag = gettag();
    const u32 krow = lane & 15;
    u32 kadr = L_K + w * 16384 + krow * 256 + ((lr ^ (((krow & 3) << 2) | ((krow >> 2) & 3))) << 4);
    const u32 q4 = (lane & 15) >> 2, p4 = lane & 3;
    u32 va[2][8];
#pragma unroll
    for (int t = 0; t < 2; ++t) {
      const u32 row = 8 * (lr & 1) + 4 * t + q4;
      const u32 base = L_V + w * 16384 + (lr >> 1) * 4096 + row * 256 + (((p4 >> 1) ^ ((q4 << 2) | (2 * (lr & 1) + t))) << 4) + 8 * (p4 & 1);
#pragma unroll
      for (int c = 0; c < 8; ++c) { va[t][c] = base ^ (c << 5); asm volatile("" : "+v"(va[t][c])); }
    }
    u32 ob = WS_OP + (((2 * g + (lane >> 5)) * 16 + s) * 32 + (lane & 31)) * 16;
    asm volatile("" : "+v"(kadr), "+v"(ob));
    VMCNT(16);
    s16x8 kf[16];
#pragma unroll
    for (int i = 0; i < 4; ++i)
#pragma unroll
      for (int st = 0; st < 4; ++st) kf[i * 4 + st] = *(const LAS s16x8*)(smem + ((kadr ^ (st << 6)) + i * 4096));
#pragma unroll
    for (int i = 0; i < 16; ++i) asm volatile("" : "+v"(kf[i]));
    add_flag(FL_MX);
    STAMP(2);
    wait_flag(FL_Q, 1);
    STAMP(3);
    s16x8 qf[4];
#pragma unroll
    for (int st = 0; st < 4; ++st) qf[st] = *(const LAS s16x8*)(smem + L_QI + (lane & 1) * 256 + st * 64 + lr * 16);
    f32x4 sc[4];
#pragma unroll
    for (int i = 0; i < 4; ++i) sc[i] = f32x4{0.f, 0.f, 0.f, 0.f};
#pragma unroll
    for (int st = 0; st < 4; ++st)
#pragma unroll
      for (int i = 0; i < 4; ++i)
        sc[i] = __builtin_amdgcn_mfma_f32_16x16x32_bf16(__builtin_bit_cast(bf16x8, qf[st]), __builtin_bit_cast(bf16x8, kf[i * 4 + st]), sc[i], 0, 0, 0);
    constexpr float C2 = 0.08838834764831845f * 1.4426950408889634f;
    const float s0 = sel4(lr, sc[0][0], sc[1][0], sc[2][0], sc[3][0]) * C2;
    const float s1 = sel4(lr, sc[0][1], sc[1][1], sc[2][1], sc[3][1]) * C2;
    LAS float* mx = (LAS float*)(smem + L_MX);
    {
      const float wm0 = wave_max(s0), wm1 = wave_max(s1);
      if (lane == 0) *(LAS v2u*)(mx + 2 * w) = v2u{__float_as_uint(wm0), __float_as_uint(wm1)};
    }
    add_flag(FL_M2);
    wait_flag_tight(FL_M2, 4);
    const f32x4 ma = *(const LAS f32x4*)(mx), mb4 = *(const LAS f32x4*)(mx + 4);
    const float m0 = r22(fmaxf(fmaxf(ma[0], ma[2]), fmaxf(mb4[0], mb4[2])));
    const float m1 = r22(fmaxf(fmaxf(ma[1], ma[3]), fmaxf(mb4[1], mb4[3])));
    const u32 pp = pk(__builtin_amdgcn_exp2f(s0 - m0), __builtin_amdgcn_exp2f(s1 - m1));
    LAS bf16_t* pi = (LAS bf16_t*)(smem + L_PI + w * 256);
    pi[lane] = (bf16_t)(pp & 0xFFFFu); pi[64 + lane] = (bf16_t)(pp >> 16);
    {
      const float l0 = wave_sum(lo2f(pp)), l1 = wave_sum(hi2f(pp));
      if (lane == 0) *(LAS v2u*)(mx + 8 + 2 * w) = v2u{__float_as_uint(l0), __float_as_uint(l1)};
    }
    s16x8 pf[2];
#pragma unroll
    for (int ks = 0; ks < 2; ++ks) pf[ks] = *(const LAS s16x8*)(smem + L_PI + w * 256 + (lane & 1) * 128 + ks * 64 + lr * 16);
    f32x4 oc[8];
#pragma unroll
    for (int c = 0; c < 8; ++c) oc[c] = f32x4{0.f, 0.f, 0.f, 0.f};
    STAMP(4);
#pragma unroll
    for (int ks = 0; ks < 2; ++ks) {
      if (ks == 0) VMCNT(8); else VMCNT(0);
      if (ks == 1) STAMP(5);
      s16x4 b0[8], b1[8];
#pragma unroll
      for (int c = 0; c < 8; ++c) {
        b0[c] = __builtin_amdgcn_ds_read_tr16_b64_v4i16((LAS s16x4*)(smem + va[0][c] + ks * 8192));
        b1[c] = __builtin_amdgcn_ds_read_tr16_b64_v4i16((LAS s16x4*)(smem + va[1][c] + ks * 8192));
      }
      SB();
#pragma unroll
      for (int c = 0; c < 8; ++c) {
        const s16x8 bb = __builtin_shufflevector(b0[c], b1[c], 0, 1, 2, 3, 4, 5, 6, 7);
        oc[c] = __builtin_amdgcn_mfma_f32_16x16x32_bf16(__builtin_bit_cast(bf16x8, pf[ks]), __builtin_bit_cast(bf16x8, bb), oc[c], 0, 0, 0);
      }
      SB();
    }
    if (lane < 16) {
      LAS float* ow = (LAS float*)(smem + L_OW) + w * 256 + lane;
#pragma unroll
      for (int c = 0; c < 8; ++c) { ow[c * 16] = oc[c][0]; ow[128 + c * 16] = oc[c][1]; }
    }
    const u32 arrived = bump_flag(FL_OW);
    STAMP(6);
    if (arrived == 3) {
      f32x4 t[4];
#pragma unroll
      for (int u = 0; u < 4; ++u) t[u] = *(const LAS f32x4*)(smem + L_OW + u * 1024 + lane * 16);
      const f32x4 la = *(const LAS f32x4*)(mx + 8), lb = *(const LAS f32x4*)(mx + 12);
      const f32x4 o = (t[0] + t[1]) + (t[2] + t[3]);
      st_quad(rws, ob, quad(o[0], o[1], o[2], o[3], tag));
      const float L = (lane >> 5) ? (la[1] + la[3]) + (lb[1] + lb[3]) : (la[0] + la[2]) + (lb[0] + lb[2]);
      st_quad(rws, (lane & 31) == 0 ? WS_ML + ((2 * g + (lane >> 5)) * 16 + s) * 16 : OOB, quad((lane >> 5) ? m1 : m0, L, 0.f, 0.f, tag));
      set_flag(FL_OPUB);
      STAMP(7);
    }
  } else if (wave < 10) {
    __builtin_amdgcn_s_setprio(1);
    const int w2 = wave & 1;
    const int r = j >> 2, c4 = j & 3;
    const rsrc_t rw = mk(a.wo, NQ * D * 2);
    const rsrc_t rws = mk(a.ws, WS_END);
    u32 vo[9];
#pragma unroll
    for (int i = 0; i < 9; ++i) {
      const u32 id = (w2 + 2 * i) * 64 + lane;
      const u32 row = (id * 3641u) >> 16, c = id - row * 18;
      vo[i] = (64 * r + row) * 1152 + c4 * 288 + c * 16;
      asm volatile("" : "+v"(vo[i]));
    }
    const Tag tag = gettag();
    wait_flag(FL_G0, 3);
    STAMP(1);
#pragma unroll
    for (int i = 0; i < 9; ++i) dma16(rw, vo[i], 0, L_WO + (w2 + 2 * i) * 1024);
    __builtin_amdgcn_s_setprio(3);
    STAMP(2);
    VMCNT(0);
    add_flag(FL_WOL);
    wait_flag(FL_WOL, 2);
    STAMP(3);
    const u32 lr = lane >> 4, q4 = (lane & 15) >> 2, p4 = lane & 3;
    const u32 badr = L_WO + (8 * lr + q4) * 288 + p4 * 8 + w2 * 32;
    s16x4 bb[5][4];
#pragma unroll
    for (int t = 0; t < 5; ++t)
#pragma unroll
      for (int u = 0; u < 4; ++u)
        bb[t][u] = __builtin_amdgcn_ds_read_tr16_b64_v4i16((LAS s16x4*)(smem + badr + ((u >> 1) * 32 + (u & 1) * 4) * 288 + ((t == 4 && w2) ? 3 : t) * 64));
    u32 offA, offB;
    {
      const u32 cqA = c4 * 36 + (w2 + 2 * lr) * 4 + q4, cqB = c4 * 36 + 32 + q4;
      offA = p4 == 0 ? WS_FIN + (((cqA >> 1) * 2) * 64 + r) * 32 + (cqA & 1) * 16 : OOB;
      offB = (w2 == 0 && lr == 0 && p4 == 0) ? WS_FIN + (((cqB >> 1) * 2) * 64 + r) * 32 + (cqB & 1) * 16 : OOB;
      asm volatile("" : "+v"(offA), "+v"(offB));
    }
#pragma unroll
    for (int t = 0; t < 5; ++t)
#pragma unroll
      for (int u = 0; u < 4; ++u) asm volatile("" : "+v"(bb[t][u]));
    wait_flag_tight(FL_O, 1);
    STAMP(4);
    const s16x8 a0 = *(const LAS s16x8*)(smem + L_OI + lr * 16), a1 = *(const LAS s16x8*)(smem + L_OI + 64 + lr * 16);
    f32x4 acc[5];
#pragma unroll
    for (int t = 0; t < 5; ++t) {
      f32x4 c = {0.f, 0.f, 0.f, 0.f};
      c = __builtin_amdgcn_mfma_f32_16x16x32_bf16(__builtin_bit_cast(bf16x8, a0), __builtin_bit_cast(bf16x8, __builtin_shufflevector(bb[t][0], bb[t][1], 0, 1, 2, 3, 4, 5, 6, 7)), c, 0, 0, 0);
      c = __builtin_amdgcn_mfma_f32_16x16x32_bf16(__builtin_bit_cast(bf16x8, a1), __builtin_bit_cast(bf16x8, __builtin_shufflevector(bb[t][2], bb[t][3], 0, 1, 2, 3, 4, 5, 6, 7)), c, 0, 0, 0);
      acc[t] = c;
    }
    st_quad(rws, offA, quad_of_lanes(sel4(lr, acc[0][0], acc[1][0], acc[2][0], acc[3][0]), tag));
    if (w2 == 0) st_quad(rws, offB, quad_of_lanes(acc[4][0], tag));
    add_flag(FL_FIN);
    STAMP(5);
  } else if (wave == 10) {
    __builtin_amdgcn_s_setprio(1);
    const int r = j >> 2, c4 = j & 3;
    const rsrc_t rw = mk(a.wd, F * D * 2);
    const rsrc_t rws = mk(a.ws, WS_END);
    u32 vo[21];
#pragma unroll
    for (int i = 0; i < 21; ++i) {
      const u32 id = i * 64 + lane;
      const u32 row = (id * 3641u) >> 16, c = id - row * 18;
      const u32 grow = row >= 64 ? 4096 + 8 * r + (row - 64) : 64 * r + row;
      vo[i] = id < 1296u ? grow * 1152 + c4 * 288 + c * 16 : OOB;
      asm volatile("" : "+v"(vo[i]));
    }
    const Tag tag = gettag();
    wait_flag(FL_G0, 3);
    wait_flag(FL_MX, 4);
    STAMP(1);
#pragma unroll
    for (int i = 0; i < 21; ++i) dma16(rw, vo[i], 0, L_K + i * 1024);
    __builtin_amdgcn_s_setprio(3);
    STAMP(2);
    VMCNT(DH_LEFT);
    *flg(FL_DH) = 1u;
    VMCNT(0);
    set_flag(FL_DL);
    STAMP(3);
    down_gemv(0, j, lane, tag, rws);
    STAMP(5);
  } else {
    __builtin_amdgcn_s_setprio(3);
    const rsrc_t rws = mk(a.ws, WS_END);
    const int h = j >> 3, hf = (j >> 2) & 1;
    const Tag tag = gettag();
    const u32 lr = lane >> 4;
    {
      const u32 base = WS_Q + (j & ~15) * 256 + lane * 16;
      wait_flag(FL_QPUB, 1);
      __builtin_amdgcn_s_sleep(Q_DELAY);
      wait_flag(FL_VH, 1);
      STAMP(1);
      v4u g[4];
      GATHER(g, 4, base + i * 1024);
      f32x4 v[4];
#pragma unroll
      for (int i = 0; i < 4; ++i) v[i] = gv4(g[i]);
      float qv[4];
#pragma unroll
      for (int k = 0; k < 4; ++k) qv[k] = swap16_add(swap32_add(v[0][k], v[2][k]), swap32_add(v[1][k], v[3][k]));
      *(LAS v2u*)(smem + L_QI + (lr * 64 + (lane & 15) * 4) * 2) = v2u{pk(qv[0], qv[1]), pk(qv[2], qv[3])};
      set_flag(FL_Q);
      STAMP(2);
    }
    {
      const u32 bo = WS_OP + ((h * 16 + lr) * 32 + hf * 16 + (lane & 15)) * 16;
      const u32 bl = WS_ML + (h * 16 + ((lane & 12) | ((lane + lr) & 3))) * 16;
      wait_flag(FL_OPUB, 1);
      __builtin_amdgcn_s_sleep(O_DELAY);
      wait_flag(FL_DH, 1);
      STAMP(5);
      v4u g[5];
      GATHER(g, 5, (i == 0 ? bl : bo + (i - 1) * 2048));
      STAMP(6);
      const float m = gv(g[0].x), l = gv(g[0].y);
      float M = m; M = fmaxf(M, dpp<DPP_XOR1>(M)); M = fmaxf(M, dpp<DPP_XOR2>(M)); M = fmaxf(M, dpp<DPP_HMIRROR>(M)); M = fmaxf(M, dpp<DPP_MIRROR>(M));
      const float wt = __builtin_amdgcn_exp2f(m - M);
      float L = wt * l; L += dpp<DPP_XOR1>(L); L += dpp<DPP_XOR2>(L); L += dpp<DPP_HMIRROR>(L); L += dpp<DPP_MIRROR>(L);
      f32x4 acc = dpp<0x150>(wt) * gv4(g[1]);
      acc += dpp<0x154>(wt) * gv4(g[2]);
      acc += dpp<0x158>(wt) * gv4(g[3]);
      acc += dpp<0x15C>(wt) * gv4(g[4]);
      const float o = swap16_add(swap32_add(acc[0], acc[2]), swap32_add(acc[1], acc[3])) * __builtin_amdgcn_rcpf(L);
      ((LAS bf16_t*)(smem + L_OI))[(lane & 15) * 4 + lr] = (bf16_t)(pk(o, o) & 0xFFFFu);
      set_flag(FL_O);
      STAMP(7);
    }
  }
  STAMP(9);
  STAMP_FLUSH();
}

}

extern "C" void kernel_launch(void* const* d_in, const int* in_sizes, int n_in, void* d_out, int out_size, void* d_ws, size_t ws_size,
                              hipStream_t stream) {
  Args a;
  a.x = (const bf16_t*)d_in[0]; a.kc = (const bf16_t*)d_in[1]; a.vc = (const bf16_t*)d_in[2];
  a.wq = (const bf16_t*)d_in[3]; a.wo = (const bf16_t*)d_in[4];
  a.wg = (const bf16_t*)d_in[6]; a.wu = (const bf16_t*)d_in[7]; a.wd = (const bf16_t*)d_in[8];
  a.out = (bf16_t*)d_out; a.ws = (unsigned char*)d_ws;
  (void)hipFuncSetAttribute((const void*)fused_kernel, hipFuncAttributeMaxDynamicSharedMemorySize, L_END);
  fused_kernel<<<256, NTHR, L_END, stream>>>(a);
}
```

```cpp
#include <hip/hip_runtime.h>
#include <hip/hip_bf16.h>
#include <stdint.h>

typedef unsigned short bf16_t;
typedef unsigned v4u __attribute__((ext_vector_type(4)));

constexpr int D = 576, HQ = 32, HKV = 16, SK = 65536, DH = 128, W = 4096, F = 4608;
constexpr int NQ = HQ * DH;

__device__ __forceinline__ float bf2f(unsigned short b) { return __uint_as_float(((unsigned)b) << 16); }
__device__ __forceinline__ unsigned short f2bf(float f) {
  unsigned u = __float_as_uint(f);
  u += 0x7FFFu + ((u >> 16) & 1u);
  return (unsigned short)(u >> 16);
}
__device__ __forceinline__ float rbf(float f) { return bf2f(f2bf(f)); }

__device__ void layernorm_lds(const bf16_t* x, float* xn, float* red) {
  int tid = threadIdx.x;
  if (tid < 64) {
    float v[9]; float s = 0.f;
    for (int i = 0; i < 9; ++i) { v[i] = bf2f(x[tid + 64 * i]); s += v[i]; }
    for (int m = 32; m >= 1; m >>= 1) s += __shfl_xor(s, m);
    float mean = s / 576.f;
    float q = 0.f;
    for (int i = 0; i < 9; ++i) { float d = v[i] - mean; q += d * d; }
    for (int m = 32; m >= 1; m >>= 1) q += __shfl_xor(q, m);
    float var = rbf(q / 576.f);
    float mb = rbf(mean);
    float den = rbf(sqrtf(rbf(var + 1e-5f)));
    for (int i = 0; i < 9; ++i) xn[tid + 64 * i] = rbf(rbf(v[i] - mb) / den);
  }
  __syncthreads();
}

__global__ void __launch_bounds__(256) k1(const bf16_t* x, const bf16_t* wq, const bf16_t* wg, const bf16_t* wu, float* q, float* g, float* u) {
  __shared__ float xn[D]; __shared__ float red[256 * 8];
  layernorm_lds(x, xn, red);
  int b = blockIdx.x; const bf16_t* Wm; float* out; int N; int c0;
  if (b < 64) { Wm = wq; out = q; N = NQ; c0 = b * 64; }
  else if (b < 64 + 72) { Wm = wg; out = g; N = F; c0 = (b - 64) * 64; }
  else { Wm = wu; out = u; N = F; c0 = (b - 136) * 64; }
  int tid = threadIdx.x; int cc = tid & 7, r0 = tid >> 3;
  float acc[8] = {0, 0, 0, 0, 0, 0, 0, 0};
  for (int r = r0; r < D; r += 32) {
    v4u w = *(const v4u*)(Wm + (size_t)r * N + c0 + cc * 8);
    float xv = xn[r];
    for (int k = 0; k < 4; ++k) { acc[2 * k] += xv * __uint_as_float(w[k] << 16); acc[2 * k + 1] += xv * __uint_as_float(w[k] & 0xffff0000u); }
  }
  for (int k = 0; k < 8; ++k) red[tid * 8 + k] = acc[k];
  __syncthreads();
  if (tid < 64) { float s = 0.f; int c = tid >> 3, k = tid & 7; for (int r = 0; r < 32; ++r) s += red[(r * 8 + c) * 8 + k]; out[c0 + tid] = rbf(s); }
}

__global__ void __launch_bounds__(256) k2(const float* q, const bf16_t* kc, const bf16_t* vc, float* pm, float* pl, float* po) {
  __shared__ float sc[2][256]; __shared__ float red[16][2][128]; __shared__ float mm[2], ll[2];
  int s = blockIdx.x, h = blockIdx.y; int tid = threadIdx.x; int c = tid & 15, r = tid >> 4;
  const bf16_t* kb = kc + ((size_t)h * SK + (SK - W) + s * 256) * DH; const bf16_t* vb = vc + ((size_t)h * SK + (SK - W) + s * 256) * DH;
  float q0[8], q1[8];
  for (int k = 0; k < 8; ++k) { q0[k] = q[(2 * h) * DH + c * 8 + k]; q1[k] = q[(2 * h + 1) * DH + c * 8 + k]; }
  for (int it = 0; it < 16; ++it) {
    int key = it * 16 + r; v4u w = *(const v4u*)(kb + (size_t)key * DH + c * 8);
    float a0 = 0.f, a1 = 0.f;
    for (int k = 0; k < 4; ++k) { float lo = __uint_as_float(w[k] << 16), hi = __uint_as_float(w[k] & 0xffff0000u); a0 += lo * q0[2 * k] + hi * q0[2 * k + 1]; a1 += lo * q1[2 * k] + hi * q1[2 * k + 1]; }
    for (int m = 8; m >= 1; m >>= 1) { a0 += __shfl_xor(a0, m); a1 += __shfl_xor(a1, m); }
    if (c == 0) { sc[0][key] = a0 * 0.08838834764831845f; sc[1][key] = a1 * 0.08838834764831845f; }
  }
  __syncthreads();
  if (tid < 128) {
    int hh = tid >> 6, l = tid & 63; float m = -INFINITY;
    for (int i = 0; i < 4; ++i) m = fmaxf(m, sc[hh][l + 64 * i]);
    for (int o = 32; o >= 1; o >>= 1) m = fmaxf(m, __shfl_xor(m, o));
    float sum = 0.f;
    for (int i = 0; i < 4; ++i) { float p = __expf(sc[hh][l + 64 * i] - m); sc[hh][l + 64 * i] = p; sum += p; }
    for (int o = 32; o >= 1; o >>= 1) sum += __shfl_xor(sum, o);
    if (l == 0) { mm[hh] = m; ll[hh] = sum; }
  }
  __syncthreads();
  float a0[8] = {0, 0, 0, 0, 0, 0, 0, 0}, a1[8] = {0, 0, 0, 0, 0, 0, 0, 0};
  for (int it = 0; it < 16; ++it) {
    int key = it * 16 + r; v4u w = *(const v4u*)(vb + (size_t)key * DH + c * 8);
    float p0 = sc[0][key], p1 = sc[1][key];
    for (int k = 0; k < 4; ++k) { float lo = __uint_as_float(w[k] << 16), hi = __uint_as_float(w[k] & 0xffff0000u); a0[2 * k] += p0 * lo; a0[2 * k + 1] += p0 * hi; a1[2 * k] += p1 * lo; a1[2 * k + 1] += p1 * hi; }
  }
  for (int k = 0; k < 8; ++k) { red[r][0][c * 8 + k] = a0[k]; red[r][1][c * 8 + k] = a1[k]; }
  __syncthreads();
  { int hh = tid >> 7, d = tid & 127; float sum = 0.f; for (int i = 0; i < 16; ++i) sum += red[i][hh][d];
    int head = 2 * h + hh; po[((size_t)head * 16 + s) * DH + d] = sum;
    if (d == 0) { pm[head * 16 + s] = mm[hh]; pl[head * 16 + s] = ll[hh]; } }
}

__global__ void __launch_bounds__(576) k3(const float* pm, const float* pl, const float* po, const bf16_t* wo, const float* g, const float* u, const bf16_t* wd, float* sp, float* fp) {
  __shared__ float vec[128]; __shared__ float red[8][576];
  int b = blockIdx.x, tid = threadIdx.x; const bf16_t* Wm; float* out;
  if (b < 32) {
    if (tid < 128) {
      float M = -INFINITY; for (int s = 0; s < 16; ++s) M = fmaxf(M, pm[b * 16 + s]);
      float L = 0.f, o = 0.f; for (int s = 0; s < 16; ++s) { float w = __expf(pm[b * 16 + s] - M); L += w * pl[b * 16 + s]; o += w * po[((size_t)b * 16 + s) * DH + tid]; }
      vec[tid] = rbf(o / L);
    }
    Wm = wo + (size_t)b * 128 * D; out = sp + b * D;
  } else {
    int bb = b - 32;
    if (tid < 128) { float gv = g[bb * 128 + tid], uv = u[bb * 128 + tid]; float sl = rbf(gv / (1.f + __expf(-gv))); vec[tid] = rbf(sl * uv); }
    Wm = wd + (size_t)bb * 128 * D; out = fp + bb * D;
  }
  __syncthreads();
  int cc = tid % 72, r0 = tid / 72; float acc[8] = {0, 0, 0, 0, 0, 0, 0, 0};
  for (int r = r0; r < 128; r += 8) {
    v4u w = *(const v4u*)(Wm + (size_t)r * D + cc * 8); float xv = vec[r];
    for (int k = 0; k < 4; ++k) { acc[2 * k] += xv * __uint_as_float(w[k] << 16); acc[2 * k + 1] += xv * __uint_as_float(w[k] & 0xffff0000u); }
  }
  for (int k = 0; k < 8; ++k) red[r0][cc * 8 + k] = acc[k];
  __syncthreads();
  float s = 0.f; for (int r = 0; r < 8; ++r) s += red[r][tid];
  out[tid] = s;
}

__global__ void __launch_bounds__(576) k4(const bf16_t* x, const float* sp, const float* fp, bf16_t* out) {
  int tid = threadIdx.x; float s = 0.f, f = 0.f;
  for (int i = 0; i < 32; ++i) s += sp[i * D + tid];
  for (int i = 0; i < 36; ++i) f += fp[i * D + tid];
  s = rbf(s); f = rbf(f); float g = rbf(f / (1.f + __expf(-f)));
  out[tid] = f2bf(bf2f(x[tid]) + rbf(s * g));
}

extern "C" void kernel_launch(void* const* d_in, const int* in_sizes, int n_in, void* d_out, int out_size, void* d_ws, size_t ws_size, hipStream_t stream) {
  const bf16_t* x = (const bf16_t*)d_in[0]; const bf16_t* kc = (const bf16_t*)d_in[1]; const bf16_t* vc = (const bf16_t*)d_in[2];
  const bf16_t* wq = (const bf16_t*)d_in[3]; const bf16_t* wo = (const bf16_t*)d_in[4];
  const bf16_t* wg = (const bf16_t*)d_in[6]; const bf16_t* wu = (const bf16_t*)d_in[7]; const bf16_t* wd = (const bf16_t*)d_in[8];
  float* ws = (float*)d_ws;
  float* q = ws; float* g = q + 4096; float* u = g + 4608; float* pm = u + 4608; float* pl = pm + 512; float* po = pl + 512; float* sp = po + 32 * 16 * 128; float* fp = sp + 32 * 576;
  (void)hipFuncSetAttribute((const void*)k1, hipFuncAttributeMaxDynamicSharedMemorySize, 140*1024);
  k1<<<208, 256, 140*1024, stream>>>(x, wq, wg, wu, q, g, u);
  k2<<<dim3(16, 16), 256, 0, stream>>>(q, kc, vc, pm, pl, po);
  k3<<<68, 576, 0, stream>>>(pm, pl, po, wo, g, u, wd, sp, fp);
  k4<<<1, 576, 0, stream>>>(x, sp, fp, (bf16_t*)d_out);
}
```

```cpp
#include <hip/hip_runtime.h>
#include <stdint.h>

typedef unsigned short bf16_t;
typedef unsigned u32;
typedef unsigned long long u64;
typedef u32 v4u __attribute__((ext_vector_type(4)));
typedef u32 v2u __attribute__((ext_vector_type(2)));
typedef float f32x4 __attribute__((ext_vector_type(4)));
typedef short s16x4 __attribute__((ext_vector_type(4)));
typedef short s16x8 __attribute__((ext_vector_type(8)));
typedef __bf16 bf16x2 __attribute__((ext_vector_type(2)));
typedef __bf16 bf16x8 __attribute__((ext_vector_type(8)));
#define LAS __attribute__((address_space(3)))
#define GAS __attribute__((address_space(1)))
typedef __amdgpu_buffer_rsrc_t rsrc_t;

namespace {

constexpr int D = 576, SK = 65536, DH = 128, WIN = 4096, F = 4608, NQ = 4096;
constexpr int NWAVES = 12, NTHR = NWAVES * 64;

constexpr u32 WS_EPOCH = 0;
constexpr u32 WS_Q = 4096;
constexpr u32 WS_GM = WS_Q + 256 * 256;
constexpr u32 WS_GP = WS_GM + 64 * 8 * 256;
constexpr u32 WS_OP = WS_GP + 256 * 256;
constexpr u32 WS_ML = WS_OP + 32 * 16 * 512;
constexpr u32 WS_FIN = WS_ML + 32 * 16 * 16;
constexpr u32 WS_END = WS_FIN + 72 * 2 * 64 * 32;
constexpr u32 WS_STAMP = 4u << 20;

constexpr u32 L_K = 0;
constexpr u32 L_V = 65536;
constexpr u32 L_WO = 131072;
constexpr u32 L_XN = L_WO + 18432;
constexpr u32 L_QI = L_XN + 2304;
constexpr u32 L_PI = L_QI + 512;
constexpr u32 L_OW = L_PI + 1024;
constexpr u32 L_HI = L_OW + 4096;
constexpr u32 L_OI = L_HI + 160;
constexpr u32 L_WT = L_OI + 128;
constexpr u32 L_MX = L_WT + 64;
constexpr u32 L_TAG = L_MX + 64;
constexpr u32 L_FLG = L_TAG + 32;
constexpr u32 L_END = L_FLG + 128;
static_assert(L_END <= 163840, "LDS");
enum { FL_Q, FL_VISS, FL_MX, FL_OW, FL_OPUB, FL_H, FL_O, FL_WOL, FL_LS, FL_FIN, FL_G0, FL_TAG, FL_W0, FL_M2, FL_QPUB, FL_HPUB, FL_DL, FL_VH, FL_DH };

#define LDPOL 2
#define DMAPOL "nt"
constexpr int AUX_NT = LDPOL, AUX_SC1 = 16;
constexpr u32 SPIN_CAP = 1u << 22;
constexpr u32 OOB = 0x7FFFFFF0u;
#define KVROT 1
#define KVTILE(t, s) ((t) * 16 + (((s) + KVROT * (t)) & 15))
#define Q_DELAY 4
#define H_DELAY 8
#define ML_DELAY 12
#define O_DELAY 8
#define F_DELAY 10
#define DH_LEFT 10
#define VH_AT 10

extern __shared__ __attribute__((aligned(16))) unsigned char smem[];

__device__ __forceinline__ float lo2f(u32 p) { return __uint_as_float(p << 16); }
__device__ __forceinline__ float hi2f(u32 p) { return __uint_as_float(p & 0xFFFF0000u); }
__device__ __forceinline__ u32 pk(float a, float b) {
  bf16x2 r = {(__bf16)a, (__bf16)b};
  return __builtin_bit_cast(u32, r);
}
__device__ __forceinline__ float rbf(float f) { return lo2f(pk(f, f)); }
__device__ __forceinline__ float sel(bool c, float a, float b) { return c ? a : b; }
__device__ __forceinline__ float sel4(u32 g, float a, float b, float c, float d) {
  const bool b0 = g & 1, b1 = g & 2;
  const float lo = b0 ? b : a, hi = b0 ? d : c;
  return b1 ? hi : lo;
}
__device__ __forceinline__ rsrc_t mk(const void* p, u32 bytes) {
  return __builtin_amdgcn_make_buffer_rsrc((void*)p, 0, bytes, 0x00020000);
}
__device__ __forceinline__ float dot2(u32 w, u32 x, float acc) {
  return __builtin_amdgcn_fdot2_f32_bf16(__builtin_bit_cast(bf16x2, w), __builtin_bit_cast(bf16x2, x), acc, false);
}
template <int CTRL> __device__ __forceinline__ float dpp(float x) {
  return __builtin_bit_cast(float, __builtin_amdgcn_mov_dpp(__builtin_bit_cast(int, x), CTRL, 0xf, 0xf, true));
}
constexpr int DPP_XOR1 = 0xB1, DPP_XOR2 = 0x4E, DPP_HMIRROR = 0x141, DPP_MIRROR = 0x140, DPP_ROR4 = 0x124, DPP_ROR8 = 0x128;
__device__ __forceinline__ float swap16_add(float a, float b) {
  auto s = __builtin_amdgcn_permlane16_swap(__float_as_uint(a), __float_as_uint(b), false, false);
  return __uint_as_float(s[0]) + __uint_as_float(s[1]);
}
__device__ __forceinline__ float swap32_add(float a, float b) {
  auto s = __builtin_amdgcn_permlane32_swap(__float_as_uint(a), __float_as_uint(b), false, false);
  return __uint_as_float(s[0]) + __uint_as_float(s[1]);
}
__device__ __forceinline__ float swap16_max(float a) {
  auto s = __builtin_amdgcn_permlane16_swap(__float_as_uint(a), __float_as_uint(a), false, false);
  return fmaxf(__uint_as_float(s[0]), __uint_as_float(s[1]));
}
__device__ __forceinline__ float swap32_max(float a) {
  auto s = __builtin_amdgcn_permlane32_swap(__float_as_uint(a), __float_as_uint(a), false, false);
  return fmaxf(__uint_as_float(s[0]), __uint_as_float(s[1]));
}
__device__ __forceinline__ float wave_sum(float x) {
  x += dpp<DPP_XOR1>(x); x += dpp<DPP_XOR2>(x); x += dpp<DPP_HMIRROR>(x); x += dpp<DPP_MIRROR>(x);
  x = swap16_add(x, x); x = swap32_add(x, x);
  return x;
}
__device__ __forceinline__ float wave_max(float x) {
  x = fmaxf(x, dpp<DPP_XOR1>(x)); x = fmaxf(x, dpp<DPP_XOR2>(x)); x = fmaxf(x, dpp<DPP_HMIRROR>(x)); x = fmaxf(x, dpp<DPP_MIRROR>(x));
  x = swap16_max(x); x = swap32_max(x);
  return x;
}

struct Tag { u32 t0, t1, t2, t3; };
__device__ __forceinline__ u32 q22(float v, u32 t) { return ((__float_as_uint(v) + 0x200u) & 0xFFFFFC00u) | t; }
__device__ __forceinline__ float r22(float v) { return __uint_as_float((__float_as_uint(v) + 0x200u) & 0xFFFFFC00u); }
__device__ __forceinline__ v4u quad(float a, float b, float c, float d, Tag t) { return v4u{q22(a, t.t0), q22(b, t.t1), q22(c, t.t2), q22(d, t.t3)}; }
__device__ __forceinline__ float gv(u32 d) { return __uint_as_float(d & 0xFFFFFC00u); }
__device__ __forceinline__ f32x4 gv4(v4u v) { return f32x4{gv(v.x), gv(v.y), gv(v.z), gv(v.w)}; }
struct Chk {
  u32 a = 0;
  __device__ __forceinline__ void add(v4u v, Tag t) {
    a = __builtin_amdgcn_bitop3_b32(v.x, t.t0, a, 0xBE); a = __builtin_amdgcn_bitop3_b32(v.y, t.t1, a, 0xBE);
    a = __builtin_amdgcn_bitop3_b32(v.z, t.t2, a, 0xBE); a = __builtin_amdgcn_bitop3_b32(v.w, t.t3, a, 0xBE);
  }
  __device__ __forceinline__ bool ok() const { return __all((a & 0x3FFu) == 0u); }
};
__device__ __forceinline__ void st_quad(rsrc_t rs, u32 off, v4u q) { __builtin_amdgcn_raw_buffer_store_b128(q, rs, off, 0, AUX_SC1); }
__device__ __forceinline__ v4u ld_quad(rsrc_t rs, u32 off) { return __builtin_amdgcn_raw_buffer_load_b128(rs, off, 0, AUX_SC1); }
#define GATHER(g, N, OFFEXPR)                                              \
  _Pragma("unroll 1") for (u32 sp_ = 0; sp_ < SPIN_CAP; ++sp_) {          \
    asm volatile("" ::: "memory");                                         \
    Chk c_;                                                                \
    _Pragma("unroll") for (int i = 0; i < N; ++i) g[i] = ld_quad(rws, OFFEXPR); \
    _Pragma("unroll") for (int i = 0; i < N; ++i) c_.add(g[i], tag);      \
    COUNT_ATTEMPT();                                                       \
    if (c_.ok()) break;                                                    \
  }

__device__ __forceinline__ volatile LAS u32* flg(int i) { return (volatile LAS u32*)(smem + L_FLG) + i; }
__device__ __forceinline__ void wait_flag(int i, u32 want) {
  volatile LAS u32* p = flg(i);
#pragma unroll 1
  for (u32 s = 0; s < SPIN_CAP; ++s) {
    if (__builtin_amdgcn_readfirstlane(*p) >= want) break;
    __builtin_amdgcn_s_sleep(1);
  }
  asm volatile("" ::: "memory");
}
__device__ __forceinline__ void wait_flag_tight(int i, u32 want) {
  volatile LAS u32* p = flg(i);
#pragma unroll 1
  for (u32 s = 0; s < SPIN_CAP; ++s) {
    if (__builtin_amdgcn_readfirstlane(*p) >= want) break;
  }
  asm volatile("" ::: "memory");
}
__device__ __forceinline__ Tag gettag() {
  wait_flag(FL_TAG, 1);
  const v4u tg = *(const LAS v4u*)(smem + L_TAG);
  Tag t{(u32)__builtin_amdgcn_readfirstlane(tg.x), (u32)__builtin_amdgcn_readfirstlane(tg.y), (u32)__builtin_amdgcn_readfirstlane(tg.z), (u32)__builtin_amdgcn_readfirstlane(tg.w)};
  asm volatile("" : "+s"(t.t0), "+s"(t.t1), "+s"(t.t2), "+s"(t.t3));
  return t;
}
__device__ __forceinline__ void set_flag(int i) {
  asm volatile("s_waitcnt lgkmcnt(0)" ::: "memory");
  *flg(i) = 1u;
  asm volatile("" ::: "memory");
}
__device__ __forceinline__ void add_flag(int i) {
  asm volatile("s_waitcnt lgkmcnt(0)" ::: "memory");
  if ((threadIdx.x & 63) == 0) __hip_atomic_fetch_add((LAS u32*)(smem + L_FLG) + i, 1u, __ATOMIC_RELAXED, __HIP_MEMORY_SCOPE_WORKGROUP);
  asm volatile("" ::: "memory");
}
__device__ __forceinline__ u32 bump_flag(int i) {
  asm volatile("s_waitcnt lgkmcnt(0)" ::: "memory");
  u32 r = 0;
  if ((threadIdx.x & 63) == 0) r = __hip_atomic_fetch_add((LAS u32*)(smem + L_FLG) + i, 1u, __ATOMIC_RELAXED, __HIP_MEMORY_SCOPE_WORKGROUP);
  r = __builtin_amdgcn_readfirstlane(r);
  asm volatile("" ::: "memory");
  return r;
}
__device__ __forceinline__ void dma16(rsrc_t rs, u32 voff, u32 soff, u32 ldsoff) {
  asm volatile("s_mov_b32 m0, %0\n\ts_nop 0\n\tbuffer_load_dwordx4 %1, %2, %3 offen " DMAPOL " lds" ::"s"(ldsoff), "v"(voff), "s"(rs), "s"(soff) : "m0", "memory");
}
#define COUNT_ATTEMPT() do { } while (0)
#define VMCNT_(n) asm volatile("s_waitcnt vmcnt(" #n ")" ::: "memory")
#define VMCNT(n) VMCNT_(n)
#define SB() __builtin_amdgcn_sched_barrier(0)
#define BARRIER() do { __builtin_amdgcn_sched_barrier(0); asm volatile("s_waitcnt lgkmcnt(0)" ::: "memory"); __builtin_amdgcn_s_barrier(); asm volatile("" ::: "memory"); __builtin_amdgcn_sched_barrier(0); } while (0)

#define STAMP(i) do { } while (0)
#define STAMP_FLUSH() do { } while (0)

struct Args {
  const bf16_t* x; const bf16_t* kc; const bf16_t* vc; const bf16_t* wq; const bf16_t* wo;
  const bf16_t* wg; const bf16_t* wu; const bf16_t* wd; bf16_t* out; unsigned char* ws;
};

__device__ __forceinline__ float silu_b(float v) {
  return rbf(v * __builtin_amdgcn_rcpf(1.f + __builtin_amdgcn_exp2f(-1.4426950408889634f * v)));
}
__device__ __forceinline__ v4u quad_of_lanes(float v, Tag tag) {
  return quad(dpp<0x00>(v), dpp<0x55>(v), dpp<0xAA>(v), dpp<0xFF>(v), tag);
}

__device__ __forceinline__ void down_gemv(int part, int j, int lane, Tag tag, rsrc_t rws) {
  const int r = j >> 2, c4 = j & 3;
  const u32 lr = lane >> 4, q4 = (lane & 15) >> 2, p4 = lane & 3;
  const u32 badr = L_K + (8 * lr + q4) * 288 + p4 * 8 + part * 96;
  const u32 row4 = 64 + 4 * lr + q4;
  const u32 badr4 = L_K + (row4 < 72 ? row4 : 71) * 288 + p4 * 8 + part * 96;
  const u32 cq = c4 * 36 + (3 * part + lr) * 4 + q4;
  u32 off = (lr < 3 && p4 == 0) ? WS_FIN + (((cq >> 1) * 2 + 1) * 64 + r) * 32 + (cq & 1) * 16 : OOB;
  asm volatile("" : "+v"(off));
  wait_flag(FL_DL, 1);
  s16x4 b[3][5];
#pragma unroll
  for (int t = 0; t < 3; ++t) {
#pragma unroll
    for (int u = 0; u < 4; ++u) b[t][u] = __builtin_amdgcn_ds_read_tr16_b64_v4i16((LAS s16x4*)(smem + badr + ((u >> 1) * 32 + (u & 1) * 4) * 288 + t * 32));
    b[t][4] = __builtin_amdgcn_ds_read_tr16_b64_v4i16((LAS s16x4*)(smem + badr4 + t * 32));
  }
#pragma unroll
  for (int t = 0; t < 3; ++t)
#pragma unroll
    for (int u = 0; u < 5; ++u) asm volatile("" : "+v"(b[t][u]));
  wait_flag_tight(FL_H, 1);
  const s16x8 a0 = *(const LAS s16x8*)(smem + L_HI + lr * 16), a1 = *(const LAS s16x8*)(smem + L_HI + 64 + lr * 16);
  const s16x4 a2 = *(const LAS s16x4*)(smem + L_HI + 128 + lr * 8);
  f32x4 acc[3];
#pragma unroll
  for (int t = 0; t < 3; ++t) {
    f32x4 c = {0.f, 0.f, 0.f, 0.f};
    c = __builtin_amdgcn_mfma_f32_16x16x16bf16_1k(a2, b[t][4], c, 0, 0, 0);
    c = __builtin_amdgcn_mfma_f32_16x16x32_bf16(__builtin_bit_cast(bf16x8, a0), __builtin_bit_cast(bf16x8, __builtin_shufflevector(b[t][0], b[t][1], 0, 1, 2, 3, 4, 5, 6, 7)), c, 0, 0, 0);
    c = __builtin_amdgcn_mfma_f32_16x16x32_bf16(__builtin_bit_cast(bf16x8, a1), __builtin_bit_cast(bf16x8, __builtin_shufflevector(b[t][2], b[t][3], 0, 1, 2, 3, 4, 5, 6, 7)), c, 0, 0, 0);
    acc[t] = c;
  }
  st_quad(rws, off, quad_of_lanes(sel4(lr, acc[0][0], acc[1][0], acc[2][0], acc[2][0]), tag));
  add_flag(FL_FIN);
}

__global__ void __launch_bounds__(NTHR, 3) fused_kernel(Args a) {
  const int tid = threadIdx.x, lane = tid & 63;
  const int wave = __builtin_amdgcn_readfirstlane(tid >> 6);
  const int b = blockIdx.x;
  const int j = (b & 7) * 32 + (b >> 3);
  STAMP(0);
  if (wave == 11) {
    if (lane < 32) *flg(lane) = 0u;
    if (lane >= 32 && lane < 36) *(LAS u32*)(smem + L_HI + 144 + (lane - 32) * 4) = 0u;
  }
  BARRIER();

  if (__builtin_expect(wave < 4, 1)) {
    __builtin_amdgcn_s_setprio(3);
    const int kp = j & 3, r = j >> 2;
    const bf16_t* wm; u32 row0, stride, strip, nrows, pub;
    if (wave == 0) { wm = a.wq; row0 = 144 * kp; stride = NQ * 2; strip = r; nrows = 144; pub = WS_Q + j * 256; }
    else if (wave < 3) {
      const int quarter = 2 * (kp & 1) + (wave - 1);
      wm = (kp >> 1) ? a.wu : a.wg; row0 = 144 * quarter; stride = F * 2; nrows = 144;
      strip = (r & ~7) | ((r + 3 * wave) & 7);
      pub = WS_GM + ((strip * 2 + (kp >> 1)) * 4 + quarter) * 256;
    } else { wm = ((j >> 4) & 1) ? a.wu : a.wg; row0 = 36 * (j & 15); stride = F * 2; strip = 64 + (j >> 5); nrows = 36; pub = WS_GP + j * 256; }
    const rsrc_t rw = mk((const unsigned char*)wm + (size_t)row0 * stride + strip * 128, (nrows - 1) * stride + 128);
    const rsrc_t rx = mk(a.x, D * 2);
    const rsrc_t rws = mk(a.ws, WS_END);
    const u64 ep = __hip_atomic_load((GAS u64*)(a.ws + WS_EPOCH), __ATOMIC_RELAXED, __HIP_MEMORY_SCOPE_AGENT);
    u32 xr[5];
#pragma unroll
    for (int i = 0; i < 5; ++i) xr[i] = __builtin_amdgcn_raw_buffer_load_b32(rx, lane * 4, i * 256, 0);
    const u32 rs8 = lane >> 3;
    const u32 voff = rs8 * stride + (lane & 7) * 16;
    u64 tg = ep;
    Tag tag;
    auto layernorm = [&](v4u (&av)[18]) {
#pragma unroll
      for (int i = 0; i < 5; ++i) asm volatile("" : "+v"(xr[i]));
      asm volatile("" : "+v"(tg));
      tg += 1;
      {
        const u64 T = ((u64)(u32)__builtin_amdgcn_readfirstlane((u32)(tg >> 32)) << 32) | (u32)__builtin_amdgcn_readfirstlane((u32)tg);
        const u64 H = T * 0x9E3779B97F4A7C15ull;
        tag = Tag{(u32)T & 0x3FFu, (u32)(H >> 54), (u32)(H >> 44) & 0x3FFu, (u32)(H >> 34) & 0x3FFu};
        if (wave == 0) {
          if (lane == 0) { *(LAS v4u*)(smem + L_TAG) = v4u{tag.t0, tag.t1, tag.t2, tag.t3}; *(LAS v2u*)(smem + L_TAG + 16) = v2u{(u32)T, (u32)(T >> 32)}; }
          set_flag(FL_TAG);
        }
      }
      float s = 0.f, q = 0.f;
#pragma unroll
      for (int i = 0; i < 5; ++i) { s = dot2(xr[i], 0x3F803F80u, s); q = dot2(xr[i], xr[i], q); }
      s = wave_sum(s); q = wave_sum(q);
      const float mean = s * (1.f / 576.f);
      const float var = rbf(fmaxf(fmaf(-mean, mean, q * (1.f / 576.f)), 0.f));
      const float mb = rbf(mean);
      const float inv = __builtin_amdgcn_rcpf(rbf(__builtin_amdgcn_sqrtf(rbf(var + 1e-5f))));
      LAS v2u* xn = (LAS v2u*)(smem + L_XN);
#pragma unroll
      for (int i = 0; i < 5; ++i) {
        const u32 d = pk(lo2f(xr[i]) - mb, hi2f(xr[i]) - mb);
        const u32 n = pk(lo2f(d) * inv, hi2f(d) * inv);
        if (i < 4 || lane < 32) xn[i * 64 + lane] = v2u{__builtin_amdgcn_perm(n, n, 0x01000100u), __builtin_amdgcn_perm(n, n, 0x03020302u)};
      }
      asm volatile("s_waitcnt lgkmcnt(0)" ::: "memory");
      u32 xp[18];
      const LAS u32* xs = (const LAS u32*)(smem + L_XN) + row0 + rs8;
#pragma unroll
      for (int i = 0; i < 18; ++i) xp[i] = xs[(wave == 3 && i > 4) ? 0 : 8 * i];
      if (wave == 3 && rs8 >= 4) xp[4] = 0u;
      u32 msk[4];
#pragma unroll
      for (int d = 0; d < 4; ++d) msk[d] = (((lane & 7) >> 1) == d) ? ((lane & 1) ? 0xFFFF0000u : 0x0000FFFFu) : 0u;
#pragma unroll
      for (int i = 0; i < 18; ++i) av[i] = v4u{xp[i] & msk[0], xp[i] & msk[1], xp[i] & msk[2], xp[i] & msk[3]};
    };
    auto gemv_publish = [&](const v4u (&av)[18], const v4u (&w)[18]) {
      f32x4 da = {0.f, 0.f, 0.f, 0.f}, db = {0.f, 0.f, 0.f, 0.f};
#pragma unroll
      for (int i = 0; i < 18; ++i) {
        if (i & 1) db = __builtin_amdgcn_mfma_f32_16x16x32_bf16(__builtin_bit_cast(bf16x8, av[i]), __builtin_bit_cast(bf16x8, w[i]), db, 0, 0, 0);
        else da = __builtin_amdgcn_mfma_f32_16x16x32_bf16(__builtin_bit_cast(bf16x8, av[i]), __builtin_bit_cast(bf16x8, w[i]), da, 0, 0, 0);
      }
      da += db;
      f32x4 res;
#pragma unroll
      for (int k = 0; k < 4; ++k) {
        const float up = dpp<DPP_ROR8>(da[k]);
        res[k] = da[k] + __uint_as_float(__builtin_amdgcn_permlane32_swap(__float_as_uint(up), __float_as_uint(up), false, false)[1]);
      }
      const u32 off = ((lane & 0x28) == 0) ? pub + (2 * (lane & 7) + (lane >> 4)) * 16 : OOB;
      st_quad(rws, off, quad(res[0], res[1], res[2], res[3], tag));
    };
    if (__builtin_expect(wave == 0, 1)) {
      v4u w[18], av[18];
#pragma unroll
      for (int i = 0; i < 18; ++i) w[i] = __builtin_amdgcn_raw_buffer_load_b128(rw, voff, 8 * i * stride, AUX_NT);
      SB();
      set_flag(FL_W0);
      STAMP(1);
      layernorm(av);
      STAMP(2);
      gemv_publish(av, w);
      set_flag(FL_QPUB);
      STAMP(3);
      {
        const u32 bm = WS_GM + r * 2048 + lane * 16;
        const u32 bp = WS_GP + ((r >> 3) * 32 + (lane >> 1)) * 256 + ((r & 7) * 2 + (lane & 1)) * 16;
        wait_flag(FL_HPUB, 3);
        __builtin_amdgcn_s_sleep(H_DELAY);
        wait_flag(FL_DH, 1);
        STAMP(4);
        v4u g[3];
        GATHER(g, 3, (i < 2 ? bm + i * 1024 : bp));
        STAMP(5);
        const f32x4 gg = gv4(g[0]), uu = gv4(g[1]), tt = gv4(g[2]);
        const bool tail = lane >= 16;
        float hv[4];
#pragma unroll
        for (int k = 0; k < 4; ++k) {
          float m = swap16_add(gg[k], uu[k]);
          m = swap32_add(m, m);
          float t = tt[k];
          t += dpp<DPP_XOR2>(t); t += dpp<DPP_ROR4>(t); t += dpp<DPP_ROR8>(t); t = swap16_add(t, t);
          const auto sm = __builtin_amdgcn_permlane16_swap(__float_as_uint(m), __float_as_uint(m), false, false);
          const auto stl = __builtin_amdgcn_permlane32_swap(__float_as_uint(t), __float_as_uint(t), false, false);
          const float gs = __uint_as_float(tail ? stl[0] : sm[0]), us = __uint_as_float(tail ? stl[1] : sm[1]);
          hv[k] = silu_b(rbf(gs)) * rbf(us);
        }
        if (lane < 18) *(LAS v2u*)(smem + L_HI + lane * 8) = v2u{pk(hv[0], hv[1]), pk(hv[2], hv[3])};
        set_flag(FL_H);
        STAMP(6);
      }
      if ((j & 31) < 9) {
        const int f = (j >> 5) * 9 + (j & 31);
        const u32 mycol = f * 8 + 4 * (lane & 1) + 2 * ((lane >> 4) & 1) + ((lane >> 3) & 1);
        const u32 xo = __builtin_amdgcn_raw_buffer_load_b16(rx, mycol * 2, 0, 0);
        const u32 base = WS_FIN + f * 4096 + lane * 16;
        wait_flag(FL_FIN, 5);
        __builtin_amdgcn_s_sleep(F_DELAY);
        STAMP(7);
        v4u g[4];
        GATHER(g, 4, base + i * 1024);
        STAMP(1);
        const f32x4 sv = gv4(g[0]) + gv4(g[1]), fv = gv4(g[2]) + gv4(g[3]);
        float a4[4];
#pragma unroll
        for (int k = 0; k < 4; ++k) a4[k] = swap32_add(sv[k], fv[k]);
        const float a0 = swap16_add(a4[0], a4[2]), a1 = swap16_add(a4[1], a4[3]);
        float b0 = a0 + dpp<DPP_XOR2>(a0), b1 = a1 + dpp<DPP_XOR2>(a1);
        b0 += dpp<DPP_ROR4>(b0); b1 += dpp<DPP_ROR4>(b1);
        b0 += dpp<DPP_ROR8>(b0); b1 += dpp<DPP_ROR8>(b1);
        const float t = (lane & 8) ? b1 : b0;
        const auto sf = __builtin_amdgcn_permlane32_swap(__float_as_uint(t), __float_as_uint(t), false, false);
        const float sb = rbf(__uint_as_float(sf[0])), fb = rbf(__uint_as_float(sf[1]));
        const float o = lo2f(xo) + rbf(sb * silu_b(fb));
        if ((lane & 0x26) == 0) *(GAS bf16_t*)((GAS unsigned char*)a.out + mycol * 2) = (bf16_t)(pk(o, o) & 0xFFFFu);
        if (f == 0 && lane == 0) { const v2u nx = *(const LAS v2u*)(smem + L_TAG + 16); __hip_atomic_store((GAS u64*)(a.ws + WS_EPOCH), (u64)nx.x | ((u64)nx.y << 32), __ATOMIC_RELAXED, __HIP_MEMORY_SCOPE_AGENT); }
        STAMP(2);
      }
    } else {
      v4u w[18], av[18];
      layernorm(av);
      STAMP(2);
      __builtin_amdgcn_s_setprio(1);
#pragma unroll
      for (int i = 0; i < 18; ++i) asm volatile("" : "+v"(av[i]));
      wait_flag(FL_VISS, 4);
#pragma unroll
      for (int i = 0; i < 18; ++i) w[i] = __builtin_amdgcn_raw_buffer_load_b128(rw, voff, 8 * i * stride, AUX_NT);
      SB();
      add_flag(FL_G0);
      __builtin_amdgcn_s_setprio(3);
      STAMP(5);
      gemv_publish(av, w);
      add_flag(FL_HPUB);
      STAMP(3);
      if (wave >= 2) {
        down_gemv(wave - 1, j, lane, tag, rws);
        STAMP(6);
      }
    }
  } else if (__builtin_expect(wave < 8, 1)) {
    __builtin_amdgcn_s_setprio(0);
    const int w = wave - 4, g = j >> 4, s = j & 15;
    const rsrc_t rk = mk(a.kc + ((size_t)g * SK + (SK - WIN)) * DH, WIN * DH * 2);
    const rsrc_t rv = mk(a.vc + ((size_t)g * SK + (SK - WIN)) * DH, WIN * DH * 2);
    const rsrc_t rws = mk(a.ws, WS_END);
    const u32 lr = lane >> 4;
    u32 vo[4];
#pragma unroll
    for (int sub = 0; sub < 4; ++sub) { vo[sub] = sub * 1024 + lr * 256 + ((((lane & 15) ^ (lr << 2)) ^ sub) << 4); asm volatile("" : "+v"(vo[sub])); }
    wait_flag(FL_W0, 1);
#pragma unroll
    for (int p = 0; p < 16; ++p) dma16(rk, vo[p & 3], KVTILE(4 * w + (p >> 2), s) * 4096, L_K + w * 16384 + p * 1024);
#pragma unroll
    for (int p = 0; p < 16; ++p) {
      dma16(rv, vo[p & 3], KVTILE(4 * w + (p >> 2), s) * 4096, L_V + w * 16384 + p * 1024);
      if (p == VH_AT && w == 0) *flg(FL_VH) = 1u;
    }
    add_flag(FL_VISS);
    __builtin_amdgcn_s_setprio(3);
    STAMP(1);
    const Tag tag = gettag();
    const u32 krow = lane & 15;
    u32 kadr = L_K + w * 16384 + krow * 256 + ((lr ^ (((krow & 3) << 2) | ((krow >> 2) & 3))) << 4);
    const u32 q4 = (lane & 15) >> 2, p4 = lane & 3;
    u32 va[2][8];
#pragma unroll
    for (int t = 0; t < 2; ++t) {
      const u32 row = 8 * (lr & 1) + 4 * t + q4;
      const u32 base = L_V + w * 16384 + (lr >> 1) * 4096 + row * 256 + (((p4 >> 1) ^ ((q4 << 2) | (2 * (lr & 1) + t))) << 4) + 8 * (p4 & 1);
#pragma unroll
      for (int c = 0; c < 8; ++c) { va[t][c] = base ^ (c << 5); asm volatile("" : "+v"(va[t][c])); }
    }
    u32 ob = WS_OP + (((2 * g + (lane >> 5)) * 16 + s) * 32 + (lane & 31)) * 16;
    asm volatile("" : "+v"(kadr), "+v"(ob));
    VMCNT(16);
    s16x8 kf[16];
#pragma unroll
    for (int i = 0; i < 4; ++i)
#pragma unroll
      for (int st = 0; st < 4; ++st) kf[i * 4 + st] = *(const LAS s16x8*)(smem + ((kadr ^ (st << 6)) + i * 4096));
#pragma unroll
    for (int i = 0; i < 16; ++i) asm volatile("" : "+v"(kf[i]));
    add_flag(FL_MX);
    STAMP(2);
    wait_flag(FL_Q, 1);
    STAMP(3);
    s16x8 qf[4];
#pragma unroll
    for (int st = 0; st < 4; ++st) qf[st] = *(const LAS s16x8*)(smem + L_QI + (lane & 1) * 256 + st * 64 + lr * 16);
    f32x4 sc[4];
#pragma unroll
    for (int i = 0; i < 4; ++i) sc[i] = f32x4{0.f, 0.f, 0.f, 0.f};
#pragma unroll
    for (int st = 0; st < 4; ++st)
#pragma unroll
      for (int i = 0; i < 4; ++i)
        sc[i] = __builtin_amdgcn_mfma_f32_16x16x32_bf16(__builtin_bit_cast(bf16x8, qf[st]), __builtin_bit_cast(bf16x8, kf[i * 4 + st]), sc[i], 0, 0, 0);
    constexpr float C2 = 0.08838834764831845f * 1.4426950408889634f;
    const float s0 = sel4(lr, sc[0][0], sc[1][0], sc[2][0], sc[3][0]) * C2;
    const float s1 = sel4(lr, sc[0][1], sc[1][1], sc[2][1], sc[3][1]) * C2;
    LAS float* mx = (LAS float*)(smem + L_MX);
    {
      const float wm0 = wave_max(s0), wm1 = wave_max(s1);
      if (lane == 0) *(LAS v2u*)(mx + 2 * w) = v2u{__float_as_uint(wm0), __float_as_uint(wm1)};
    }
    add_flag(FL_M2);
    wait_flag_tight(FL_M2, 4);
    const f32x4 ma = *(const LAS f32x4*)(mx), mb4 = *(const LAS f32x4*)(mx + 4);
    const float m0 = r22(fmaxf(fmaxf(ma[0], ma[2]), fmaxf(mb4[0], mb4[2])));
    const float m1 = r22(fmaxf(fmaxf(ma[1], ma[3]), fmaxf(mb4[1], mb4[3])));
    const u32 pp = pk(__builtin_amdgcn_exp2f(s0 - m0), __builtin_amdgcn_exp2f(s1 - m1));
    LAS bf16_t* pi = (LAS bf16_t*)(smem + L_PI + w * 256);
    pi[lane] = (bf16_t)(pp & 0xFFFFu); pi[64 + lane] = (bf16_t)(pp >> 16);
    {
      const float l0 = wave_sum(lo2f(pp)), l1 = wave_sum(hi2f(pp));
      if (lane == 0) *(LAS v2u*)(mx + 8 + 2 * w) = v2u{__float_as_uint(l0), __float_as_uint(l1)};
    }
    s16x8 pf[2];
#pragma unroll
    for (int ks = 0; ks < 2; ++ks) pf[ks] = *(const LAS s16x8*)(smem + L_PI + w * 256 + (lane & 1) * 128 + ks * 64 + lr * 16);
    f32x4 oc[8];
#pragma unroll
    for (int c = 0; c < 8; ++c) oc[c] = f32x4{0.f, 0.f, 0.f, 0.f};
    STAMP(4);
#pragma unroll
    for (int ks = 0; ks < 2; ++ks) {
      if (ks == 0) VMCNT(8); else VMCNT(0);
      if (ks == 1) STAMP(5);
      s16x4 b0[8], b1[8];
#pragma unroll
      for (int c = 0; c < 8; ++c) {
        b0[c] = __builtin_amdgcn_ds_read_tr16_b64_v4i16((LAS s16x4*)(smem + va[0][c] + ks * 8192));
        b1[c] = __builtin_amdgcn_ds_read_tr16_b64_v4i16((LAS s16x4*)(smem + va[1][c] + ks * 8192));
      }
      SB();
#pragma unroll
      for (int c = 0; c < 8; ++c) {
        const s16x8 bb = __builtin_shufflevector(b0[c], b1[c], 0, 1, 2, 3, 4, 5, 6, 7);
        oc[c] = __builtin_amdgcn_mfma_f32_16x16x32_bf16(__builtin_bit_cast(bf16x8, pf[ks]), __builtin_bit_cast(bf16x8, bb), oc[c], 0, 0, 0);
      }
      SB();
    }
    if (lane < 16) {
      LAS float* ow = (LAS float*)(smem + L_OW) + w * 256 + lane;
#pragma unroll
      for (int c = 0; c < 8; ++c) { ow[c * 16] = oc[c][0]; ow[128 + c * 16] = oc[c][1]; }
    }
    const u32 arrived = bump_flag(FL_OW);
    STAMP(6);
    if (arrived == 3) {
      f32x4 t[4];
#pragma unroll
      for (int u = 0; u < 4; ++u) t[u] = *(const LAS f32x4*)(smem + L_OW + u * 1024 + lane * 16);
      const f32x4 la = *(const LAS f32x4*)(mx + 8), lb = *(const LAS f32x4*)(mx + 12);
      const f32x4 o = (t[0] + t[1]) + (t[2] + t[3]);
      st_quad(rws, ob, quad(o[0], o[1], o[2], o[3], tag));
      const float L = (lane >> 5) ? (la[1] + la[3]) + (lb[1] + lb[3]) : (la[0] + la[2]) + (lb[0] + lb[2]);
      st_quad(rws, (lane & 31) == 0 ? WS_ML + ((2 * g + (lane >> 5)) * 16 + s) * 16 : OOB, quad((lane >> 5) ? m1 : m0, L, 0.f, 0.f, tag));
      set_flag(FL_OPUB);
      STAMP(7);
    }
  } else if (wave < 10) {
    __builtin_amdgcn_s_setprio(1);
    const int w2 = wave & 1;
    const int r = j >> 2, c4 = j & 3;
    const rsrc_t rw = mk(a.wo, NQ * D * 2);
    const rsrc_t rws = mk(a.ws, WS_END);
    u32 vo[9];
#pragma unroll
    for (int i = 0; i < 9; ++i) {
      const u32 id = (w2 + 2 * i) * 64 + lane;
      const u32 row = (id * 3641u) >> 16, c = id - row * 18;
      vo[i] = (64 * r + row) * 1152 + c4 * 288 + c * 16;
      asm volatile("" : "+v"(vo[i]));
    }
    const Tag tag = gettag();
    wait_flag(FL_G0, 3);
    STAMP(1);
#pragma unroll
    for (int i = 0; i < 9; ++i) dma16(rw, vo[i], 0, L_WO + (w2 + 2 * i) * 1024);
    __builtin_amdgcn_s_setprio(3);
    STAMP(2);
    VMCNT(0);
    add_flag(FL_WOL);
    wait_flag(FL_WOL, 2);
    STAMP(3);
    const u32 lr = lane >> 4, q4 = (lane & 15) >> 2, p4 = lane & 3;
    const u32 badr = L_WO + (8 * lr + q4) * 288 + p4 * 8 + w2 * 32;
    s16x4 bb[5][4];
#pragma unroll
    for (int t = 0; t < 5; ++t)
#pragma unroll
      for (int u = 0; u < 4; ++u)
        bb[t][u] = __builtin_amdgcn_ds_read_tr16_b64_v4i16((LAS s16x4*)(smem + badr + ((u >> 1) * 32 + (u & 1) * 4) * 288 + ((t == 4 && w2) ? 3 : t) * 64));
    u32 offA, offB;
    {
      const u32 cqA = c4 * 36 + (w2 + 2 * lr) * 4 + q4, cqB = c4 * 36 + 32 + q4;
      offA = p4 == 0 ? WS_FIN + (((cqA >> 1) * 2) * 64 + r) * 32 + (cqA & 1) * 16 : OOB;
      offB = (w2 == 0 && lr == 0 && p4 == 0) ? WS_FIN + (((cqB >> 1) * 2) * 64 + r) * 32 + (cqB & 1) * 16 : OOB;
      asm volatile("" : "+v"(offA), "+v"(offB));
    }
#pragma unroll
    for (int t = 0; t < 5; ++t)
#pragma unroll
      for (int u = 0; u < 4; ++u) asm volatile("" : "+v"(bb[t][u]));
    wait_flag_tight(FL_O, 1);
    STAMP(4);
    const s16x8 a0 = *(const LAS s16x8*)(smem + L_OI + lr * 16), a1 = *(const LAS s16x8*)(smem + L_OI + 64 + lr * 16);
    f32x4 acc[5];
#pragma unroll
    for (int t = 0; t < 5; ++t) {
      f32x4 c = {0.f, 0.f, 0.f, 0.f};
      c = __builtin_amdgcn_mfma_f32_16x16x32_bf16(__builtin_bit_cast(bf16x8, a0), __builtin_bit_cast(bf16x8, __builtin_shufflevector(bb[t][0], bb[t][1], 0, 1, 2, 3, 4, 5, 6, 7)), c, 0, 0, 0);
      c = __builtin_amdgcn_mfma_f32_16x16x32_bf16(__builtin_bit_cast(bf16x8, a1), __builtin_bit_cast(bf16x8, __builtin_shufflevector(bb[t][2], bb[t][3], 0, 1, 2, 3, 4, 5, 6, 7)), c, 0, 0, 0);
      acc[t] = c;
    }
    st_quad(rws, offA, quad_of_lanes(sel4(lr, acc[0][0], acc[1][0], acc[2][0], acc[3][0]), tag));
    if (w2 == 0) st_quad(rws, offB, quad_of_lanes(acc[4][0], tag));
    add_flag(FL_FIN);
    STAMP(5);
  } else if (wave == 10) {
    __builtin_amdgcn_s_setprio(1);
    const int r = j >> 2, c4 = j & 3;
    const rsrc_t rw = mk(a.wd, F * D * 2);
    const rsrc_t rws = mk(a.ws, WS_END);
    u32 vo[21];
#pragma unroll
    for (int i = 0; i < 21; ++i) {
      const u32 id = i * 64 + lane;
      const u32 row = (id * 3641u) >> 16, c = id - row * 18;
      const u32 grow = row >= 64 ? 4096 + 8 * r + (row - 64) : 64 * r + row;
      vo[i] = id < 1296u ? grow * 1152 + c4 * 288 + c * 16 : OOB;
      asm volatile("" : "+v"(vo[i]));
    }
    const Tag tag = gettag();
    wait_flag(FL_G0, 3);
    wait_flag(FL_MX, 4);
    STAMP(1);
#pragma unroll
    for (int i = 0; i < 21; ++i) dma16(rw, vo[i], 0, L_K + i * 1024);
    __builtin_amdgcn_s_setprio(3);
    STAMP(2);
    VMCNT(DH_LEFT);
    *flg(FL_DH) = 1u;
    VMCNT(0);
    set_flag(FL_DL);
    STAMP(3);
    down_gemv(0, j, lane, tag, rws);
    STAMP(5);
  } else {
    __builtin_amdgcn_s_setprio(3);
    const rsrc_t rws = mk(a.ws, WS_END);
    const int h = j >> 3, hf = (j >> 2) & 1;
    const Tag tag = gettag();
    const u32 lr = lane >> 4;
    {
      const u32 base = WS_Q + (j & ~15) * 256 + lane * 16;
      wait_flag(FL_QPUB, 1);
      __builtin_amdgcn_s_sleep(Q_DELAY);
      wait_flag(FL_VH, 1);
      STAMP(1);
      v4u g[4];
      GATHER(g, 4, base + i * 1024);
      f32x4 v[4];
#pragma unroll
      for (int i = 0; i < 4; ++i) v[i] = gv4(g[i]);
      float qv[4];
#pragma unroll
      for (int k = 0; k < 4; ++k) qv[k] = swap16_add(swap32_add(v[0][k], v[2][k]), swap32_add(v[1][k], v[3][k]));
      *(LAS v2u*)(smem + L_QI + (lr * 64 + (lane & 15) * 4) * 2) = v2u{pk(qv[0], qv[1]), pk(qv[2], qv[3])};
      set_flag(FL_Q);
      STAMP(2);
    }
    {
      const u32 bo = WS_OP + ((h * 16 + lr) * 32 + hf * 16 + (lane & 15)) * 16;
      const u32 bl = WS_ML + (h * 16 + ((lane & 12) | ((lane + lr) & 3))) * 16;
      wait_flag(FL_OPUB, 1);
      __builtin_amdgcn_s_sleep(O_DELAY);
      wait_flag(FL_DH, 1);
      STAMP(5);
      v4u g[5];
      GATHER(g, 5, (i == 0 ? bl : bo + (i - 1) * 2048));
      STAMP(6);
      const float m = gv(g[0].x), l = gv(g[0].y);
      float M = m; M = fmaxf(M, dpp<DPP_XOR1>(M)); M = fmaxf(M, dpp<DPP_XOR2>(M)); M = fmaxf(M, dpp<DPP_HMIRROR>(M)); M = fmaxf(M, dpp<DPP_MIRROR>(M));
      const float wt = __builtin_amdgcn_exp2f(m - M);
      float L = wt * l; L += dpp<DPP_XOR1>(L); L += dpp<DPP_XOR2>(L); L += dpp<DPP_HMIRROR>(L); L += dpp<DPP_MIRROR>(L);
      f32x4 acc = dpp<0x150>(wt) * gv4(g[1]);
      acc += dpp<0x154>(wt) * gv4(g[2]);
      acc += dpp<0x158>(wt) * gv4(g[3]);
      acc += dpp<0x15C>(wt) * gv4(g[4]);
      const float o = swap16_add(swap32_add(acc[0], acc[2]), swap32_add(acc[1], acc[3])) * __builtin_amdgcn_rcpf(L);
      ((LAS bf16_t*)(smem + L_OI))[(lane & 15) * 4 + lr] = (bf16_t)(pk(o, o) & 0xFFFFu);
      set_flag(FL_O);
      STAMP(7);
    }
  }
  STAMP(9);
  STAMP_FLUSH();
}

}

extern "C" void kernel_launch(void* const* d_in, const int* in_sizes, int n_in, void* d_out, int out_size, void* d_ws, size_t ws_size,
                              hipStream_t stream) {
  Args a;
  a.x = (const bf16_t*)d_in[0]; a.kc = (const bf16_t*)d_in[1]; a.vc = (const bf16_t*)d_in[2];
  a.wq = (const bf16_t*)d_in[3]; a.wo = (const bf16_t*)d_in[4];
  a.wg = (const bf16_t*)d_in[6]; a.wu = (const bf16_t*)d_in[7]; a.wd = (const bf16_t*)d_in[8];
  a.out = (bf16_t*)d_out; a.ws = (unsigned char*)d_ws;
  (void)hipFuncSetAttribute((const void*)fused_kernel, hipFuncAttributeMaxDynamicSharedMemorySize, L_END);
  fused_kernel<<<256, NTHR, L_END, stream>>>(a);
}
```

```cpp
#define K_GATE 0
#define TLBPF
#include <hip/hip_runtime.h>
#include <stdint.h>

typedef unsigned short bf16_t;
typedef unsigned u32;
typedef unsigned long long u64;
typedef u32 v4u __attribute__((ext_vector_type(4)));
typedef u32 v2u __attribute__((ext_vector_type(2)));
typedef float f32x4 __attribute__((ext_vector_type(4)));
typedef short s16x4 __attribute__((ext_vector_type(4)));
typedef short s16x8 __attribute__((ext_vector_type(8)));
typedef __bf16 bf16x2 __attribute__((ext_vector_type(2)));
typedef __bf16 bf16x8 __attribute__((ext_vector_type(8)));
#define LAS __attribute__((address_space(3)))
#define GAS __attribute__((address_space(1)))
typedef __amdgpu_buffer_rsrc_t rsrc_t;

namespace {

constexpr int D = 576, SK = 65536, DH = 128, WIN = 4096, F = 4608, NQ = 4096;
constexpr int NWAVES = 12, NTHR = NWAVES * 64;

constexpr u32 WS_EPOCH = 0;
constexpr u32 WS_Q = 4096;
constexpr u32 WS_GM = WS_Q + 256 * 256;
constexpr u32 WS_GP = WS_GM + 64 * 8 * 256;
constexpr u32 WS_OP = WS_GP + 256 * 256;
constexpr u32 WS_ML = WS_OP + 32 * 16 * 512;
constexpr u32 WS_FIN = WS_ML + 32 * 16 * 16;
constexpr u32 WS_END = WS_FIN + 72 * 2 * 64 * 32;
constexpr u32 WS_STAMP = 4u << 20;

constexpr u32 L_K = 0;
constexpr u32 L_V = 65536;
constexpr u32 L_WO = 131072;
constexpr u32 L_XN = L_WO + 18432;
constexpr u32 L_QI = L_XN + 2304;
constexpr u32 L_PI = L_QI + 512;
constexpr u32 L_OW = L_PI + 1024;
constexpr u32 L_HI = L_OW + 4096;
constexpr u32 L_OI = L_HI + 160;
constexpr u32 L_WT = L_OI + 128;
constexpr u32 L_MX = L_WT + 64;
constexpr u32 L_TAG = L_MX + 64;
constexpr u32 L_FLG = L_TAG + 32;
constexpr u32 L_END = L_FLG + 128;
static_assert(L_END <= 163840, "LDS");
enum { FL_Q, FL_VISS, FL_MX, FL_OW, FL_OPUB, FL_H, FL_O, FL_WOL, FL_LS, FL_FIN, FL_G0, FL_TAG, FL_W0, FL_M2, FL_QPUB, FL_HPUB, FL_DL, FL_VH, FL_DH };

#define LDPOL 2
#define DMAPOL "nt"
constexpr int AUX_NT = LDPOL, AUX_SC1 = 16;
constexpr u32 SPIN_CAP = 1u << 22;
constexpr u32 OOB = 0x7FFFFFF0u;
#define KVROT 1
#define KVTILE(t, s) ((t) * 16 + (((s) + KVROT * (t)) & 15))
#define THR_KV 63
#define THR_GU 63
#define THR_WO 63
#define THR_D 63
template <int N> __device__ __forceinline__ void vm_throttle() { if constexpr (N < 63) asm volatile("s_waitcnt vmcnt(%0)" ::"n"(N) : "memory"); }
#define Q_DELAY 4
#define H_DELAY 8
#define ML_DELAY 12
#define O_DELAY 8
#define F_DELAY 10
#define DH_LEFT 10
#define VH_AT 10

extern __shared__ __attribute__((aligned(16))) unsigned char smem[];

__device__ __forceinline__ float lo2f(u32 p) { return __uint_as_float(p << 16); }
__device__ __forceinline__ float hi2f(u32 p) { return __uint_as_float(p & 0xFFFF0000u); }
__device__ __forceinline__ u32 pk(float a, float b) {
  bf16x2 r = {(__bf16)a, (__bf16)b};
  return __builtin_bit_cast(u32, r);
}
__device__ __forceinline__ float rbf(float f) { return lo2f(pk(f, f)); }
__device__ __forceinline__ float sel(bool c, float a, float b) { return c ? a : b; }
__device__ __forceinline__ float sel4(u32 g, float a, float b, float c, float d) {
  const bool b0 = g & 1, b1 = g & 2;
  const float lo = b0 ? b : a, hi = b0 ? d : c;
  return b1 ? hi : lo;
}
__device__ __forceinline__ rsrc_t mk(const void* p, u32 bytes) {
  return __builtin_amdgcn_make_buffer_rsrc((void*)p, 0, bytes, 0x00020000);
}
__device__ __forceinline__ float dot2(u32 w, u32 x, float acc) {
  return __builtin_amdgcn_fdot2_f32_bf16(__builtin_bit_cast(bf16x2, w), __builtin_bit_cast(bf16x2, x), acc, false);
}
template <int CTRL> __device__ __forceinline__ float dpp(float x) {
  return __builtin_bit_cast(float, __builtin_amdgcn_mov_dpp(__builtin_bit_cast(int, x), CTRL, 0xf, 0xf, true));
}
constexpr int DPP_XOR1 = 0xB1, DPP_XOR2 = 0x4E, DPP_HMIRROR = 0x141, DPP_MIRROR = 0x140, DPP_ROR4 = 0x124, DPP_ROR8 = 0x128;
__device__ __forceinline__ float swap16_add(float a, float b) {
  auto s = __builtin_amdgcn_permlane16_swap(__float_as_uint(a), __float_as_uint(b), false, false);
  return __uint_as_float(s[0]) + __uint_as_float(s[1]);
}
__device__ __forceinline__ float swap32_add(float a, float b) {
  auto s = __builtin_amdgcn_permlane32_swap(__float_as_uint(a), __float_as_uint(b), false, false);
  return __uint_as_float(s[0]) + __uint_as_float(s[1]);
}
__device__ __forceinline__ float swap16_max(float a) {
  auto s = __builtin_amdgcn_permlane16_swap(__float_as_uint(a), __float_as_uint(a), false, false);
  return fmaxf(__uint_as_float(s[0]), __uint_as_float(s[1]));
}
__device__ __forceinline__ float swap32_max(float a) {
  auto s = __builtin_amdgcn_permlane32_swap(__float_as_uint(a), __float_as_uint(a), false, false);
  return fmaxf(__uint_as_float(s[0]), __uint_as_float(s[1]));
}
__device__ __forceinline__ float wave_sum(float x) {
  x += dpp<DPP_XOR1>(x); x += dpp<DPP_XOR2>(x); x += dpp<DPP_HMIRROR>(x); x += dpp<DPP_MIRROR>(x);
  x = swap16_add(x, x); x = swap32_add(x, x);
  return x;
}
__device__ __forceinline__ float wave_max(float x) {
  x = fmaxf(x, dpp<DPP_XOR1>(x)); x = fmaxf(x, dpp<DPP_XOR2>(x)); x = fmaxf(x, dpp<DPP_HMIRROR>(x)); x = fmaxf(x, dpp<DPP_MIRROR>(x));
  x = swap16_max(x); x = swap32_max(x);
  return x;
}

struct Tag { u32 t0, t1, t2, t3; };
__device__ __forceinline__ u32 q22(float v, u32 t) { return ((__float_as_uint(v) + 0x200u) & 0xFFFFFC00u) | t; }
__device__ __forceinline__ float r22(float v) { return __uint_as_float((__float_as_uint(v) + 0x200u) & 0xFFFFFC00u); }
__device__ __forceinline__ v4u quad(float a, float b, float c, float d, Tag t) { return v4u{q22(a, t.t0), q22(b, t.t1), q22(c, t.t2), q22(d, t.t3)}; }
__device__ __forceinline__ float gv(u32 d) { return __uint_as_float(d & 0xFFFFFC00u); }
__device__ __forceinline__ f32x4 gv4(v4u v) { return f32x4{gv(v.x), gv(v.y), gv(v.z), gv(v.w)}; }
struct Chk {
  u32 a = 0;
  __device__ __forceinline__ void add(v4u v, Tag t) {
    a = __builtin_amdgcn_bitop3_b32(v.x, t.t0, a, 0xBE); a = __builtin_amdgcn_bitop3_b32(v.y, t.t1, a, 0xBE);
    a = __builtin_amdgcn_bitop3_b32(v.z, t.t2, a, 0xBE); a = __builtin_amdgcn_bitop3_b32(v.w, t.t3, a, 0xBE);
  }
  __device__ __forceinline__ bool ok() const { return __all((a & 0x3FFu) == 0u); }
};
__device__ __forceinline__ void st_quad(rsrc_t rs, u32 off, v4u q) { __builtin_amdgcn_raw_buffer_store_b128(q, rs, off, 0, AUX_SC1); }
__device__ __forceinline__ v4u ld_quad(rsrc_t rs, u32 off) { return __builtin_amdgcn_raw_buffer_load_b128(rs, off, 0, AUX_SC1); }
#define GATHER(g, N, OFFEXPR)                                              \
  _Pragma("unroll 1") for (u32 sp_ = 0; sp_ < SPIN_CAP; ++sp_) {          \
    asm volatile("" ::: "memory");                                         \
    Chk c_;                                                                \
    _Pragma("unroll") for (int i = 0; i < N; ++i) g[i] = ld_quad(rws, OFFEXPR); \
    _Pragma("unroll") for (int i = 0; i < N; ++i) c_.add(g[i], tag);      \
    COUNT_ATTEMPT();                                                       \
    if (c_.ok()) break;                                                    \
  }

__device__ __forceinline__ volatile LAS u32* flg(int i) { return (volatile LAS u32*)(smem + L_FLG) + i; }
__device__ __forceinline__ void wait_flag(int i, u32 want) {
  volatile LAS u32* p = flg(i);
#pragma unroll 1
  for (u32 s = 0; s < SPIN_CAP; ++s) {
    if (__builtin_amdgcn_readfirstlane(*p) >= want) break;
    __builtin_amdgcn_s_sleep(1);
  }
  asm volatile("" ::: "memory");
}
__device__ __forceinline__ void wait_flag_tight(int i, u32 want) {
  volatile LAS u32* p = flg(i);
#pragma unroll 1
  for (u32 s = 0; s < SPIN_CAP; ++s) {
    if (__builtin_amdgcn_readfirstlane(*p) >= want) break;
  }
  asm volatile("" ::: "memory");
}
__device__ __forceinline__ Tag gettag() {
  wait_flag(FL_TAG, 1);
  const v4u tg = *(const LAS v4u*)(smem + L_TAG);
  Tag t{(u32)__builtin_amdgcn_readfirstlane(tg.x), (u32)__builtin_amdgcn_readfirstlane(tg.y), (u32)__builtin_amdgcn_readfirstlane(tg.z), (u32)__builtin_amdgcn_readfirstlane(tg.w)};
  asm volatile("" : "+s"(t.t0), "+s"(t.t1), "+s"(t.t2), "+s"(t.t3));
  return t;
}
__device__ __forceinline__ void set_flag(int i) {
  asm volatile("s_waitcnt lgkmcnt(0)" ::: "memory");
  *flg(i) = 1u;
  asm volatile("" ::: "memory");
}
__device__ __forceinline__ void add_flag(int i) {
  asm volatile("s_waitcnt lgkmcnt(0)" ::: "memory");
  if ((threadIdx.x & 63) == 0) __hip_atomic_fetch_add((LAS u32*)(smem + L_FLG) + i, 1u, __ATOMIC_RELAXED, __HIP_MEMORY_SCOPE_WORKGROUP);
  asm volatile("" ::: "memory");
}
__device__ __forceinline__ u32 bump_flag(int i) {
  asm volatile("s_waitcnt lgkmcnt(0)" ::: "memory");
  u32 r = 0;
  if ((threadIdx.x & 63) == 0) r = __hip_atomic_fetch_add((LAS u32*)(smem + L_FLG) + i, 1u, __ATOMIC_RELAXED, __HIP_MEMORY_SCOPE_WORKGROUP);
  r = __builtin_amdgcn_readfirstlane(r);
  asm volatile("" ::: "memory");
  return r;
}
__device__ __forceinline__ void dma16(rsrc_t rs, u32 voff, u32 soff, u32 ldsoff) {
  asm volatile("s_mov_b32 m0, %0\n\ts_nop 0\n\tbuffer_load_dwordx4 %1, %2, %3 offen " DMAPOL " lds" ::"s"(ldsoff), "v"(voff), "s"(rs), "s"(soff) : "m0", "memory");
}
#define G2POL "nt"
__device__ __forceinline__ void dma16b(rsrc_t rs, u32 voff, u32 soff, u32 ldsoff) {
  asm volatile("s_mov_b32 m0, %0\n\ts_nop 0\n\tbuffer_load_dwordx4 %1, %2, %3 offen " G2POL " lds" ::"s"(ldsoff), "v"(voff), "s"(rs), "s"(soff) : "m0", "memory");
}
#define COUNT_ATTEMPT() do { } while (0)
#define VMCNT_(n) asm volatile("s_waitcnt vmcnt(" #n ")" ::: "memory")
#define VMCNT(n) VMCNT_(n)
#define SB() __builtin_amdgcn_sched_barrier(0)
#define BARRIER() do { __builtin_amdgcn_sched_barrier(0); asm volatile("s_waitcnt lgkmcnt(0)" ::: "memory"); __builtin_amdgcn_s_barrier(); asm volatile("" ::: "memory"); __builtin_amdgcn_sched_barrier(0); } while (0)

#define STAMP(i) do { } while (0)
#define STAMP_FLUSH() do { } while (0)

struct Args {
  const bf16_t* x; const bf16_t* kc; const bf16_t* vc; const bf16_t* wq; const bf16_t* wo;
  const bf16_t* wg; const bf16_t* wu; const bf16_t* wd; bf16_t* out; unsigned char* ws;
};

__device__ __forceinline__ float silu_b(float v) {
  return rbf(v * __builtin_amdgcn_rcpf(1.f + __builtin_amdgcn_exp2f(-1.4426950408889634f * v)));
}
__device__ __forceinline__ v4u quad_of_lanes(float v, Tag tag) {
  return quad(dpp<0x00>(v), dpp<0x55>(v), dpp<0xAA>(v), dpp<0xFF>(v), tag);
}

__device__ __forceinline__ void down_gemv(int part, int j, int lane, Tag tag, rsrc_t rws) {
  const int r = j >> 2, c4 = j & 3;
  const u32 lr = lane >> 4, q4 = (lane & 15) >> 2, p4 = lane & 3;
  const u32 badr = L_K + (8 * lr + q4) * 288 + p4 * 8 + part * 96;
  const u32 row4 = 64 + 4 * lr + q4;
  const u32 badr4 = L_K + (row4 < 72 ? row4 : 71) * 288 + p4 * 8 + part * 96;
  const u32 cq = c4 * 36 + (3 * part + lr) * 4 + q4;
  u32 off = (lr < 3 && p4 == 0) ? WS_FIN + (((cq >> 1) * 2 + 1) * 64 + r) * 32 + (cq & 1) * 16 : OOB;
  asm volatile("" : "+v"(off));
  wait_flag(FL_DL, 1);
  s16x4 b[3][5];
#pragma unroll
  for (int t = 0; t < 3; ++t) {
#pragma unroll
    for (int u = 0; u < 4; ++u) b[t][u] = __builtin_amdgcn_ds_read_tr16_b64_v4i16((LAS s16x4*)(smem + badr + ((u >> 1) * 32 + (u & 1) * 4) * 288 + t * 32));
    b[t][4] = __builtin_amdgcn_ds_read_tr16_b64_v4i16((LAS s16x4*)(smem + badr4 + t * 32));
  }
#pragma unroll
  for (int t = 0; t < 3; ++t)
#pragma unroll
    for (int u = 0; u < 5; ++u) asm volatile("" : "+v"(b[t][u]));
  wait_flag_tight(FL_H, 1);
  const s16x8 a0 = *(const LAS s16x8*)(smem + L_HI + lr * 16), a1 = *(const LAS s16x8*)(smem + L_HI + 64 + lr * 16);
  const s16x4 a2 = *(const LAS s16x4*)(smem + L_HI + 128 + lr * 8);
  f32x4 acc[3];
#pragma unroll
  for (int t = 0; t < 3; ++t) {
    f32x4 c = {0.f, 0.f, 0.f, 0.f};
    c = __builtin_amdgcn_mfma_f32_16x16x16bf16_1k(a2, b[t][4], c, 0, 0, 0);
    c = __builtin_amdgcn_mfma_f32_16x16x32_bf16(__builtin_bit_cast(bf16x8, a0), __builtin_bit_cast(bf16x8, __builtin_shufflevector(b[t][0], b[t][1], 0, 1, 2, 3, 4, 5, 6, 7)), c, 0, 0, 0);
    c = __builtin_amdgcn_mfma_f32_16x16x32_bf16(__builtin_bit_cast(bf16x8, a1), __builtin_bit_cast(bf16x8, __builtin_shufflevector(b[t][2], b[t][3], 0, 1, 2, 3, 4, 5, 6, 7)), c, 0, 0, 0);
    acc[t] = c;
  }
  st_quad(rws, off, quad_of_lanes(sel4(lr, acc[0][0], acc[1][0], acc[2][0], acc[2][0]), tag));
  add_flag(FL_FIN);
}

__global__ void __launch_bounds__(NTHR, 3) fused_kernel(Args a) {
  const int tid = threadIdx.x, lane = tid & 63;
  const int wave = __builtin_amdgcn_readfirstlane(tid >> 6);
  const int b = blockIdx.x;
  const int j = (b & 7) * 32 + (b >> 3);
  STAMP(0);
  if (wave == 11) {
    if (lane < 32) *flg(lane) = 0u;
    if (lane >= 32 && lane < 36) *(LAS u32*)(smem + L_HI + 144 + (lane - 32) * 4) = 0u;
  }
  BARRIER();

  if (__builtin_expect(wave < 4, 1)) {
    __builtin_amdgcn_s_setprio(3);
    const int kp = j & 3, r = j >> 2;
    const bf16_t* wm; u32 row0, stride, strip, nrows, pub;
    if (wave == 0) { wm = a.wq; row0 = 144 * kp; stride = NQ * 2; strip = r; nrows = 144; pub = WS_Q + j * 256; }
    else if (wave < 3) {
      const int quarter = 2 * (kp & 1) + (wave - 1);
      wm = (kp >> 1) ? a.wu : a.wg; row0 = 144 * quarter; stride = F * 2; nrows = 144;
      strip = (r & ~7) | ((r + 3 * wave) & 7);
      pub = WS_GM + ((strip * 2 + (kp >> 1)) * 4 + quarter) * 256;
    } else { wm = ((j >> 4) & 1) ? a.wu : a.wg; row0 = 36 * (j & 15); stride = F * 2; strip = 64 + (j >> 5); nrows = 36; pub = WS_GP + j * 256; }
    const rsrc_t rw = mk((const unsigned char*)wm + (size_t)row0 * stride + strip * 128, (nrows - 1) * stride + 128);
    const rsrc_t rx = mk(a.x, D * 2);
    const rsrc_t rws = mk(a.ws, WS_END);
    const u64 ep = __hip_atomic_load((GAS u64*)(a.ws + WS_EPOCH), __ATOMIC_RELAXED, __HIP_MEMORY_SCOPE_AGENT);
    u32 xr[5];
#pragma unroll
    for (int i = 0; i < 5; ++i) xr[i] = __builtin_amdgcn_raw_buffer_load_b32(rx, lane * 4, i * 256, 0);
    const u32 rs8 = lane >> 3;
    const u32 voff = rs8 * stride + (lane & 7) * 16;
    u64 tg = ep;
    Tag tag;
    auto layernorm = [&](v4u (&av)[18]) {
#pragma unroll
      for (int i = 0; i < 5; ++i) asm volatile("" : "+v"(xr[i]));
      asm volatile("" : "+v"(tg));
      tg += 1;
      {
        const u64 T = ((u64)(u32)__builtin_amdgcn_readfirstlane((u32)(tg >> 32)) << 32) | (u32)__builtin_amdgcn_readfirstlane((u32)tg);
        const u64 H = T * 0x9E3779B97F4A7C15ull;
        tag = Tag{(u32)T & 0x3FFu, (u32)(H >> 54), (u32)(H >> 44) & 0x3FFu, (u32)(H >> 34) & 0x3FFu};
        if (wave == 0) {
          if (lane == 0) { *(LAS v4u*)(smem + L_TAG) = v4u{tag.t0, tag.t1, tag.t2, tag.t3}; *(LAS v2u*)(smem + L_TAG + 16) = v2u{(u32)T, (u32)(T >> 32)}; }
          set_flag(FL_TAG);
        }
      }
      float s = 0.f, q = 0.f;
#pragma unroll
      for (int i = 0; i < 5; ++i) { s = dot2(xr[i], 0x3F803F80u, s); q = dot2(xr[i], xr[i], q); }
      s = wave_sum(s); q = wave_sum(q);
      const float mean = s * (1.f / 576.f);
      const float var = rbf(fmaxf(fmaf(-mean, mean, q * (1.f / 576.f)), 0.f));
      const float mb = rbf(mean);
      const float inv = __builtin_amdgcn_rcpf(rbf(__builtin_amdgcn_sqrtf(rbf(var + 1e-5f))));
      LAS v2u* xn = (LAS v2u*)(smem + L_XN);
#pragma unroll
      for (int i = 0; i < 5; ++i) {
        const u32 d = pk(lo2f(xr[i]) - mb, hi2f(xr[i]) - mb);
        const u32 n = pk(lo2f(d) * inv, hi2f(d) * inv);
        if (i < 4 || lane < 32) xn[i * 64 + lane] = v2u{__builtin_amdgcn_perm(n, n, 0x01000100u), __builtin_amdgcn_perm(n, n, 0x03020302u)};
      }
      asm volatile("s_waitcnt lgkmcnt(0)" ::: "memory");
      u32 xp[18];
      const LAS u32* xs = (const LAS u32*)(smem + L_XN) + row0 + rs8;
#pragma unroll
      for (int i = 0; i < 18; ++i) xp[i] = xs[(wave == 3 && i > 4) ? 0 : 8 * i];
      if (wave == 3 && rs8 >= 4) xp[4] = 0u;
      u32 msk[4];
#pragma unroll
      for (int d = 0; d < 4; ++d) msk[d] = (((lane & 7) >> 1) == d) ? ((lane & 1) ? 0xFFFF0000u : 0x0000FFFFu) : 0u;
#pragma unroll
      for (int i = 0; i < 18; ++i) av[i] = v4u{xp[i] & msk[0], xp[i] & msk[1], xp[i] & msk[2], xp[i] & msk[3]};
    };
    auto gemv_publish = [&](const v4u (&av)[18], const v4u (&w)[18]) {
      f32x4 da = {0.f, 0.f, 0.f, 0.f}, db = {0.f, 0.f, 0.f, 0.f};
#pragma unroll
      for (int i = 0; i < 18; ++i) {
        if (i & 1) db = __builtin_amdgcn_mfma_f32_16x16x32_bf16(__builtin_bit_cast(bf16x8, av[i]), __builtin_bit_cast(bf16x8, w[i]), db, 0, 0, 0);
        else da = __builtin_amdgcn_mfma_f32_16x16x32_bf16(__builtin_bit_cast(bf16x8, av[i]), __builtin_bit_cast(bf16x8, w[i]), da, 0, 0, 0);
      }
      da += db;
      f32x4 res;
#pragma unroll
      for (int k = 0; k < 4; ++k) {
        const float up = dpp<DPP_ROR8>(da[k]);
        res[k] = da[k] + __uint_as_float(__builtin_amdgcn_permlane32_swap(__float_as_uint(up), __float_as_uint(up), false, false)[1]);
      }
      const u32 off = ((lane & 0x28) == 0) ? pub + (2 * (lane & 7) + (lane >> 4)) * 16 : OOB;
      st_quad(rws, off, quad(res[0], res[1], res[2], res[3], tag));
    };
    if (__builtin_expect(wave == 0, 1)) {
      v4u w[18], av[18];
#pragma unroll
      for (int i = 0; i < 18; ++i) w[i] = __builtin_amdgcn_raw_buffer_load_b128(rw, voff, 8 * i * stride, AUX_NT);
      SB();
      set_flag(FL_W0);
      STAMP(1);
      layernorm(av);
      STAMP(2);
      gemv_publish(av, w);
      set_flag(FL_QPUB);
      STAMP(3);
      {
        const u32 bm = WS_GM + r * 2048 + lane * 16;
        const u32 bp = WS_GP + ((r >> 3) * 32 + (lane >> 1)) * 256 + ((r & 7) * 2 + (lane & 1)) * 16;
        wait_flag(FL_HPUB, 3);
        __builtin_amdgcn_s_sleep(H_DELAY);
        wait_flag(FL_DH, 1);
        STAMP(4);
        v4u g[3];
        GATHER(g, 3, (i < 2 ? bm + i * 1024 : bp));
        STAMP(5);
        const f32x4 gg = gv4(g[0]), uu = gv4(g[1]), tt = gv4(g[2]);
        const bool tail = lane >= 16;
        float hv[4];
#pragma unroll
        for (int k = 0; k < 4; ++k) {
          float m = swap16_add(gg[k], uu[k]);
          m = swap32_add(m, m);
          float t = tt[k];
          t += dpp<DPP_XOR2>(t); t += dpp<DPP_ROR4>(t); t += dpp<DPP_ROR8>(t); t = swap16_add(t, t);
          const auto sm = __builtin_amdgcn_permlane16_swap(__float_as_uint(m), __float_as_uint(m), false, false);
          const auto stl = __builtin_amdgcn_permlane32_swap(__float_as_uint(t), __float_as_uint(t), false, false);
          const float gs = __uint_as_float(tail ? stl[0] : sm[0]), us = __uint_as_float(tail ? stl[1] : sm[1]);
          hv[k] = silu_b(rbf(gs)) * rbf(us);
        }
        if (lane < 18) *(LAS v2u*)(smem + L_HI + lane * 8) = v2u{pk(hv[0], hv[1]), pk(hv[2], hv[3])};
        set_flag(FL_H);
        STAMP(6);
      }
      if ((j & 31) < 9) {
        const int f = (j >> 5) * 9 + (j & 31);
        const u32 mycol = f * 8 + 4 * (lane & 1) + 2 * ((lane >> 4) & 1) + ((lane >> 3) & 1);
        const u32 xo = __builtin_amdgcn_raw_buffer_load_b16(rx, mycol * 2, 0, 0);
        const u32 base = WS_FIN + f * 4096 + lane * 16;
        wait_flag(FL_FIN, 5);
        __builtin_amdgcn_s_sleep(F_DELAY);
        STAMP(7);
        v4u g[4];
        GATHER(g, 4, base + i * 1024);
        STAMP(1);
        const f32x4 sv = gv4(g[0]) + gv4(g[1]), fv = gv4(g[2]) + gv4(g[3]);
        float a4[4];
#pragma unroll
        for (int k = 0; k < 4; ++k) a4[k] = swap32_add(sv[k], fv[k]);
        const float a0 = swap16_add(a4[0], a4[2]), a1 = swap16_add(a4[1], a4[3]);
        float b0 = a0 + dpp<DPP_XOR2>(a0), b1 = a1 + dpp<DPP_XOR2>(a1);
        b0 += dpp<DPP_ROR4>(b0); b1 += dpp<DPP_ROR4>(b1);
        b0 += dpp<DPP_ROR8>(b0); b1 += dpp<DPP_ROR8>(b1);
        const float t = (lane & 8) ? b1 : b0;
        const auto sf = __builtin_amdgcn_permlane32_swap(__float_as_uint(t), __float_as_uint(t), false, false);
        const float sb = rbf(__uint_as_float(sf[0])), fb = rbf(__uint_as_float(sf[1]));
        const float o = lo2f(xo) + rbf(sb * silu_b(fb));
        if ((lane & 0x26) == 0) *(GAS bf16_t*)((GAS unsigned char*)a.out + mycol * 2) = (bf16_t)(pk(o, o) & 0xFFFFu);
        if (f == 0 && lane == 0) { const v2u nx = *(const LAS v2u*)(smem + L_TAG + 16); __hip_atomic_store((GAS u64*)(a.ws + WS_EPOCH), (u64)nx.x | ((u64)nx.y << 32), __ATOMIC_RELAXED, __HIP_MEMORY_SCOPE_AGENT); }
        STAMP(2);
      }
    } else {
      v4u w[18], av[18];
      layernorm(av);
      STAMP(2);
      __builtin_amdgcn_s_setprio(1);
#pragma unroll
      for (int i = 0; i < 18; ++i) asm volatile("" : "+v"(av[i]));
      wait_flag(FL_VISS, 4);
#pragma unroll
      for (int i = 0; i < 18; ++i) { vm_throttle<THR_GU>(); SB(); w[i] = __builtin_amdgcn_raw_buffer_load_b128(rw, voff, 8 * i * stride, AUX_NT); SB(); }
      SB();
      add_flag(FL_G0);
      __builtin_amdgcn_s_setprio(3);
      STAMP(5);
      gemv_publish(av, w);
      add_flag(FL_HPUB);
      STAMP(3);
      if (wave >= 2) {
        down_gemv(wave - 1, j, lane, tag, rws);
        STAMP(6);
      }
    }
  } else if (__builtin_expect(wave < 8, 1)) {
    __builtin_amdgcn_s_setprio(0);
    const int w = wave - 4, g = j >> 4, s = j & 15;
    const rsrc_t rk = mk(a.kc + ((size_t)g * SK + (SK - WIN)) * DH, WIN * DH * 2);
    const rsrc_t rv = mk(a.vc + ((size_t)g * SK + (SK - WIN)) * DH, WIN * DH * 2);
    const rsrc_t rws = mk(a.ws, WS_END);
    const u32 lr = lane >> 4;
    u32 vo[4];
#pragma unroll
    for (int sub = 0; sub < 4; ++sub) { vo[sub] = sub * 1024 + lr * 256 + ((((lane & 15) ^ (lr << 2)) ^ sub) << 4); asm volatile("" : "+v"(vo[sub])); }
#pragma unroll
    for (int p = 0; p < 16; ++p) { vm_throttle<THR_KV>(); dma16(rk, vo[p & 3], KVTILE(4 * w + (p >> 2), s) * 4096, L_K + w * 16384 + p * 1024); }
#pragma unroll
    for (int p = 0; p < 16; ++p) {
      vm_throttle<THR_KV>();
      dma16(rv, vo[p & 3], KVTILE(4 * w + (p >> 2), s) * 4096, L_V + w * 16384 + p * 1024);
      if (p == VH_AT && w == 0) *flg(FL_VH) = 1u;
    }
    add_flag(FL_VISS);
    __builtin_amdgcn_s_setprio(3);
    STAMP(1);
    const Tag tag = gettag();
    const u32 krow = lane & 15;
    u32 kadr = L_K + w * 16384 + krow * 256 + ((lr ^ (((krow & 3) << 2) | ((krow >> 2) & 3))) << 4);
    const u32 q4 = (lane & 15) >> 2, p4 = lane & 3;
    u32 va[2][8];
#pragma unroll
    for (int t = 0; t < 2; ++t) {
      const u32 row = 8 * (lr & 1) + 4 * t + q4;
      const u32 base = L_V + w * 16384 + (lr >> 1) * 4096 + row * 256 + (((p4 >> 1) ^ ((q4 << 2) | (2 * (lr & 1) + t))) << 4) + 8 * (p4 & 1);
#pragma unroll
      for (int c = 0; c < 8; ++c) { va[t][c] = base ^ (c << 5); asm volatile("" : "+v"(va[t][c])); }
    }
    u32 ob = WS_OP + (((2 * g + (lane >> 5)) * 16 + s) * 32 + (lane & 31)) * 16;
    asm volatile("" : "+v"(kadr), "+v"(ob));
    VMCNT(16);
    s16x8 kf[16];
#pragma unroll
    for (int i = 0; i < 4; ++i)
#pragma unroll
      for (int st = 0; st < 4; ++st) kf[i * 4 + st] = *(const LAS s16x8*)(smem + ((kadr ^ (st << 6)) + i * 4096));
#pragma unroll
    for (int i = 0; i < 16; ++i) asm volatile("" : "+v"(kf[i]));
    add_flag(FL_MX);
    STAMP(2);
    wait_flag(FL_Q, 1);
    STAMP(3);
    s16x8 qf[4];
#pragma unroll
    for (int st = 0; st < 4; ++st) qf[st] = *(const LAS s16x8*)(smem + L_QI + (lane & 1) * 256 + st * 64 + lr * 16);
    f32x4 sc[4];
#pragma unroll
    for (int i = 0; i < 4; ++i) sc[i] = f32x4{0.f, 0.f, 0.f, 0.f};
#pragma unroll
    for (int st = 0; st < 4; ++st)
#pragma unroll
      for (int i = 0; i < 4; ++i)
        sc[i] = __builtin_amdgcn_mfma_f32_16x16x32_bf16(__builtin_bit_cast(bf16x8, qf[st]), __builtin_bit_cast(bf16x8, kf[i * 4 + st]), sc[i], 0, 0, 0);
    constexpr float C2 = 0.08838834764831845f * 1.4426950408889634f;
    const float s0 = sel4(lr, sc[0][0], sc[1][0], sc[2][0], sc[3][0]) * C2;
    const float s1 = sel4(lr, sc[0][1], sc[1][1], sc[2][1], sc[3][1]) * C2;
    LAS float* mx = (LAS float*)(smem + L_MX);
    {
      const float wm0 = wave_max(s0), wm1 = wave_max(s1);
      if (lane == 0) *(LAS v2u*)(mx + 2 * w) = v2u{__float_as_uint(wm0), __float_as_uint(wm1)};
    }
    add_flag(FL_M2);
    wait_flag_tight(FL_M2, 4);
    const f32x4 ma = *(const LAS f32x4*)(mx), mb4 = *(const LAS f32x4*)(mx + 4);
    const float m0 = r22(fmaxf(fmaxf(ma[0], ma[2]), fmaxf(mb4[0], mb4[2])));
    const float m1 = r22(fmaxf(fmaxf(ma[1], ma[3]), fmaxf(mb4[1], mb4[3])));
    const u32 pp = pk(__builtin_amdgcn_exp2f(s0 - m0), __builtin_amdgcn_exp2f(s1 - m1));
    LAS bf16_t* pi = (LAS bf16_t*)(smem + L_PI + w * 256);
    pi[lane] = (bf16_t)(pp & 0xFFFFu); pi[64 + lane] = (bf16_t)(pp >> 16);
    {
      const float l0 = wave_sum(lo2f(pp)), l1 = wave_sum(hi2f(pp));
      if (lane == 0) *(LAS v2u*)(mx + 8 + 2 * w) = v2u{__float_as_uint(l0), __float_as_uint(l1)};
    }
    s16x8 pf[2];
#pragma unroll
    for (int ks = 0; ks < 2; ++ks) pf[ks] = *(const LAS s16x8*)(smem + L_PI + w * 256 + (lane & 1) * 128 + ks * 64 + lr * 16);
    f32x4 oc[8];
#pragma unroll
    for (int c = 0; c < 8; ++c) oc[c] = f32x4{0.f, 0.f, 0.f, 0.f};
    STAMP(4);
#pragma unroll
    for (int ks = 0; ks < 2; ++ks) {
      if (ks == 0) VMCNT(8); else VMCNT(0);
      if (ks == 1) STAMP(5);
      s16x4 b0[8], b1[8];
#pragma unroll
      for (int c = 0; c < 8; ++c) {
        b0[c] = __builtin_amdgcn_ds_read_tr16_b64_v4i16((LAS s16x4*)(smem + va[0][c] + ks * 8192));
        b1[c] = __builtin_amdgcn_ds_read_tr16_b64_v4i16((LAS s16x4*)(smem + va[1][c] + ks * 8192));
      }
      SB();
#pragma unroll
      for (int c = 0; c < 8; ++c) {
        const s16x8 bb = __builtin_shufflevector(b0[c], b1[c], 0, 1, 2, 3, 4, 5, 6, 7);
        oc[c] = __builtin_amdgcn_mfma_f32_16x16x32_bf16(__builtin_bit_cast(bf16x8, pf[ks]), __builtin_bit_cast(bf16x8, bb), oc[c], 0, 0, 0);
      }
      SB();
    }
    if (lane < 16) {
      LAS float* ow = (LAS float*)(smem + L_OW) + w * 256 + lane;
#pragma unroll
      for (int c = 0; c < 8; ++c) { ow[c * 16] = oc[c][0]; ow[128 + c * 16] = oc[c][1]; }
    }
    const u32 arrived = bump_flag(FL_OW);
    STAMP(6);
    if (arrived == 3) {
      f32x4 t[4];
#pragma unroll
      for (int u = 0; u < 4; ++u) t[u] = *(const LAS f32x4*)(smem + L_OW + u * 1024 + lane * 16);
      const f32x4 la = *(const LAS f32x4*)(mx + 8), lb = *(const LAS f32x4*)(mx + 12);
      const f32x4 o = (t[0] + t[1]) + (t[2] + t[3]);
      st_quad(rws, ob, quad(o[0], o[1], o[2], o[3], tag));
      const float L = (lane >> 5) ? (la[1] + la[3]) + (lb[1] + lb[3]) : (la[0] + la[2]) + (lb[0] + lb[2]);
      st_quad(rws, (lane & 31) == 0 ? WS_ML + ((2 * g + (lane >> 5)) * 16 + s) * 16 : OOB, quad((lane >> 5) ? m1 : m0, L, 0.f, 0.f, tag));
      set_flag(FL_OPUB);
      STAMP(7);
    }
  } else if (wave < 10) {
    __builtin_amdgcn_s_setprio(1);
    u32 pf_dummy = 0;
    if (wave == 8) {
      const int k = j & 31, x = j >> 5;
      const unsigned char* p = nullptr;
      if (k < 2) p = (const unsigned char*)(a.vc + ((size_t)(2 * x + k) * SK + (SK - WIN)) * DH);
      else if (k < 4) p = (const unsigned char*)(a.kc + ((size_t)(2 * x + k - 2) * SK + (SK - WIN)) * DH);
      else if (k < 7) p = (const unsigned char*)a.wg + (size_t)(k - 4) * (2u << 20) + 1024 * x;
      else if (k < 10) p = (const unsigned char*)a.wu + (size_t)(k - 7) * (2u << 20) + 1024 * x;
      else if (k < 12) p = (const unsigned char*)a.wo + (size_t)(512 * x + 511 * (k - 10)) * 1152;
      else if (k < 14) p = (const unsigned char*)a.wd + (size_t)(512 * x + 511 * (k - 12)) * 1152;
      else if (k == 14) p = (const unsigned char*)a.wd + (size_t)(4096 + 64 * x) * 1152;
      else if (k < 18) p = (const unsigned char*)a.wq + (size_t)(k - 15) * (2u << 20) + 1024 * x;
      else if (k == 18) p = a.ws + WS_FIN + 4096 * x;
      else if (k == 19) p = (const unsigned char*)a.out;
      if (p != nullptr) pf_dummy = *(const GAS u32*)p;
    }
    const int w2 = wave & 1;
    const int r = j >> 2, c4 = j & 3;
    const rsrc_t rw = mk(a.wo, NQ * D * 2);
    const rsrc_t rws = mk(a.ws, WS_END);
    u32 vo[9];
#pragma unroll
    for (int i = 0; i < 9; ++i) {
      const u32 id = (w2 + 2 * i) * 64 + lane;
      const u32 row = (id * 3641u) >> 16, c = id - row * 18;
      vo[i] = (64 * r + row) * 1152 + c4 * 288 + c * 16;
      asm volatile("" : "+v"(vo[i]));
    }
    const Tag tag = gettag();
    asm volatile("" : "+v"(pf_dummy));
    wait_flag(FL_G0, 3);
    STAMP(1);
#pragma unroll
    for (int i = 0; i < 9; ++i) { vm_throttle<THR_WO>(); dma16b(rw, vo[i], 0, L_WO + (w2 + 2 * i) * 1024); }
    __builtin_amdgcn_s_setprio(3);
    STAMP(2);
    VMCNT(0);
    add_flag(FL_WOL);
    wait_flag(FL_WOL, 2);
    STAMP(3);
    const u32 lr = lane >> 4, q4 = (lane & 15) >> 2, p4 = lane & 3;
    const u32 badr = L_WO + (8 * lr + q4) * 288 + p4 * 8 + w2 * 32;
    s16x4 bb[5][4];
#pragma unroll
    for (int t = 0; t < 5; ++t)
#pragma unroll
      for (int u = 0; u < 4; ++u)
        bb[t][u] = __builtin_amdgcn_ds_read_tr16_b64_v4i16((LAS s16x4*)(smem + badr + ((u >> 1) * 32 + (u & 1) * 4) * 288 + ((t == 4 && w2) ? 3 : t) * 64));
    u32 offA, offB;
    {
      const u32 cqA = c4 * 36 + (w2 + 2 * lr) * 4 + q4, cqB = c4 * 36 + 32 + q4;
      offA = p4 == 0 ? WS_FIN + (((cqA >> 1) * 2) * 64 + r) * 32 + (cqA & 1) * 16 : OOB;
      offB = (w2 == 0 && lr == 0 && p4 == 0) ? WS_FIN + (((cqB >> 1) * 2) * 64 + r) * 32 + (cqB & 1) * 16 : OOB;
      asm volatile("" : "+v"(offA), "+v"(offB));
    }
#pragma unroll
    for (int t = 0; t < 5; ++t)
#pragma unroll
      for (int u = 0; u < 4; ++u) asm volatile("" : "+v"(bb[t][u]));
    wait_flag_tight(FL_O, 1);
    STAMP(4);
    const s16x8 a0 = *(const LAS s16x8*)(smem + L_OI + lr * 16), a1 = *(const LAS s16x8*)(smem + L_OI + 64 + lr * 16);
    f32x4 acc[5];
#pragma unroll
    for (int t = 0; t < 5; ++t) {
      f32x4 c = {0.f, 0.f, 0.f, 0.f};
      c = __builtin_amdgcn_mfma_f32_16x16x32_bf16(__builtin_bit_cast(bf16x8, a0), __builtin_bit_cast(bf16x8, __builtin_shufflevector(bb[t][0], bb[t][1], 0, 1, 2, 3, 4, 5, 6, 7)), c, 0, 0, 0);
      c = __builtin_amdgcn_mfma_f32_16x16x32_bf16(__builtin_bit_cast(bf16x8, a1), __builtin_bit_cast(bf16x8, __builtin_shufflevector(bb[t][2], bb[t][3], 0, 1, 2, 3, 4, 5, 6, 7)), c, 0, 0, 0);
      acc[t] = c;
    }
    st_quad(rws, offA, quad_of_lanes(sel4(lr, acc[0][0], acc[1][0], acc[2][0], acc[3][0]), tag));
    if (w2 == 0) st_quad(rws, offB, quad_of_lanes(acc[4][0], tag));
    add_flag(FL_FIN);
    STAMP(5);
  } else if (wave == 10) {
    __builtin_amdgcn_s_setprio(1);
    const int r = j >> 2, c4 = j & 3;
    const rsrc_t rw = mk(a.wd, F * D * 2);
    const rsrc_t rws = mk(a.ws, WS_END);
    u32 vo[21];
#pragma unroll
    for (int i = 0; i < 21; ++i) {
      const u32 id = i * 64 + lane;
      const u32 row = (id * 3641u) >> 16, c = id - row * 18;
      const u32 grow = row >= 64 ? 4096 + 8 * r + (row - 64) : 64 * r + row;
      vo[i] = id < 1296u ? grow * 1152 + c4 * 288 + c * 16 : OOB;
      asm volatile("" : "+v"(vo[i]));
    }
    const Tag tag = gettag();
    wait_flag(FL_G0, 3);
    wait_flag(FL_MX, 4);
    STAMP(1);
#pragma unroll
    for (int i = 0; i < 21; ++i) { vm_throttle<THR_D>(); dma16b(rw, vo[i], 0, L_K + i * 1024); }
    __builtin_amdgcn_s_setprio(3);
    STAMP(2);
    VMCNT(DH_LEFT);
    *flg(FL_DH) = 1u;
    VMCNT(0);
    set_flag(FL_DL);
    STAMP(3);
    down_gemv(0, j, lane, tag, rws);
    STAMP(5);
  } else {
    __builtin_amdgcn_s_setprio(3);
    const rsrc_t rws = mk(a.ws, WS_END);
    const int h = j >> 3, hf = (j >> 2) & 1;
    const Tag tag = gettag();
    const u32 lr = lane >> 4;
    {
      const u32 base = WS_Q + (j & ~15) * 256 + lane * 16;
      wait_flag(FL_QPUB, 1);
      __builtin_amdgcn_s_sleep(Q_DELAY);
      wait_flag(FL_VH, 1);
      STAMP(1);
      v4u g[4];
      GATHER(g, 4, base + i * 1024);
      f32x4 v[4];
#pragma unroll
      for (int i = 0; i < 4; ++i) v[i] = gv4(g[i]);
      float qv[4];
#pragma unroll
      for (int k = 0; k < 4; ++k) qv[k] = swap16_add(swap32_add(v[0][k], v[2][k]), swap32_add(v[1][k], v[3][k]));
      *(LAS v2u*)(smem + L_QI + (lr * 64 + (lane & 15) * 4) * 2) = v2u{pk(qv[0], qv[1]), pk(qv[2], qv[3])};
      set_flag(FL_Q);
      STAMP(2);
    }
    {
      const u32 bo = WS_OP + ((h * 16 + lr) * 32 + hf * 16 + (lane & 15)) * 16;
      const u32 bl = WS_ML + (h * 16 + ((lane & 12) | ((lane + lr) & 3))) * 16;
      wait_flag(FL_OPUB, 1);
      __builtin_amdgcn_s_sleep(O_DELAY);
      wait_flag(FL_DH, 1);
      STAMP(5);
      v4u g[5];
      GATHER(g, 5, (i == 0 ? bl : bo + (i - 1) * 2048));
      STAMP(6);
      const float m = gv(g[0].x), l = gv(g[0].y);
      float M = m; M = fmaxf(M, dpp<DPP_XOR1>(M)); M = fmaxf(M, dpp<DPP_XOR2>(M)); M = fmaxf(M, dpp<DPP_HMIRROR>(M)); M = fmaxf(M, dpp<DPP_MIRROR>(M));
      const float wt = __builtin_amdgcn_exp2f(m - M);
      float L = wt * l; L += dpp<DPP_XOR1>(L); L += dpp<DPP_XOR2>(L); L += dpp<DPP_HMIRROR>(L); L += dpp<DPP_MIRROR>(L);
      f32x4 acc = dpp<0x150>(wt) * gv4(g[1]);
      acc += dpp<0x154>(wt) * gv4(g[2]);
      acc += dpp<0x158>(wt) * gv4(g[3]);
      acc += dpp<0x15C>(wt) * gv4(g[4]);
      const float o = swap16_add(swap32_add(acc[0], acc[2]), swap32_add(acc[1], acc[3])) * __builtin_amdgcn_rcpf(L);
      ((LAS bf16_t*)(smem + L_OI))[(lane & 15) * 4 + lr] = (bf16_t)(pk(o, o) & 0xFFFFu);
      set_flag(FL_O);
      STAMP(7);
    }
  }
  STAMP(9);
  STAMP_FLUSH();
}

}

extern "C" void kernel_launch(void* const* d_in, const int* in_sizes, int n_in, void* d_out, int out_size, void* d_ws, size_t ws_size,
                              hipStream_t stream) {
  Args a;
  a.x = (const bf16_t*)d_in[0]; a.kc = (const bf16_t*)d_in[1]; a.vc = (const bf16_t*)d_in[2];
  a.wq = (const bf16_t*)d_in[3]; a.wo = (const bf16_t*)d_in[4];
  a.wg = (const bf16_t*)d_in[6]; a.wu = (const bf16_t*)d_in[7]; a.wd = (const bf16_t*)d_in[8];
  a.out = (bf16_t*)d_out; a.ws = (unsigned char*)d_ws;
  (void)hipFuncSetAttribute((const void*)fused_kernel, hipFuncAttributeMaxDynamicSharedMemorySize, L_END);
  fused_kernel<<<256, NTHR, L_END, stream>>>(a);
}
```

```cpp
#define PIPE 0
#define K_GATE 6
#define WSPF 0
#define TLBPF
#include <hip/hip_runtime.h>
#include <stdint.h>

typedef unsigned short bf16_t;
typedef unsigned u32;
typedef unsigned long long u64;
typedef u32 v4u __attribute__((ext_vector_type(4)));
typedef u32 v2u __attribute__((ext_vector_type(2)));
typedef float f32x4 __attribute__((ext_vector_type(4)));
typedef short s16x4 __attribute__((ext_vector_type(4)));
typedef short s16x8 __attribute__((ext_vector_type(8)));
typedef __bf16 bf16x2 __attribute__((ext_vector_type(2)));
typedef __bf16 bf16x8 __attribute__((ext_vector_type(8)));
#define LAS __attribute__((address_space(3)))
#define GAS __attribute__((address_space(1)))
typedef __amdgpu_buffer_rsrc_t rsrc_t;

namespace {

constexpr int D = 576, SK = 65536, DH = 128, WIN = 4096, F = 4608, NQ = 4096;
constexpr int NWAVES = 12, NTHR = NWAVES * 64;

constexpr u32 WS_EPOCH = 0;
constexpr u32 WS_Q = 4096;
constexpr u32 WS_GM = WS_Q + 256 * 256;
constexpr u32 WS_GP = WS_GM + 64 * 8 * 256;
constexpr u32 WS_OP = WS_GP + 256 * 256;
constexpr u32 WS_ML = WS_OP + 32 * 16 * 512;
constexpr u32 WS_FIN = WS_ML + 32 * 16 * 16;
constexpr u32 WS_END = WS_FIN + 72 * 2 * 64 * 32;
constexpr u32 WS_STAMP = 4u << 20;

constexpr u32 L_K = 0;
constexpr u32 L_V = 65536;
constexpr u32 L_WO = 131072;
constexpr u32 L_XN = L_WO + 18432;
constexpr u32 L_QI = L_XN + 2304;
constexpr u32 L_PI = L_QI + 512;
constexpr u32 L_OW = L_PI + 1024;
constexpr u32 L_HI = L_OW + 4096;
constexpr u32 L_OI = L_HI + 160;
constexpr u32 L_WT = L_OI + 128;
constexpr u32 L_MX = L_WT + 64;
constexpr u32 L_TAG = L_MX + 64;
constexpr u32 L_FLG = L_TAG + 32;
constexpr u32 L_END = L_FLG + 128;
static_assert(L_END <= 163840, "LDS");
enum { FL_Q, FL_VISS, FL_MX, FL_OW, FL_OPUB, FL_H, FL_O, FL_WOL, FL_LS, FL_FIN, FL_G0, FL_TAG, FL_W0, FL_M2, FL_QPUB, FL_HPUB, FL_DL, FL_VH, FL_DH };

#define LDPOL 2
#define DMAPOL "nt"
constexpr int AUX_NT = LDPOL, AUX_SC1 = 16;
constexpr u32 SPIN_CAP = 1u << 20;
constexpr u32 OOB = 0x7FFFFFF0u;
#define KVROT 1
#define KVTILE(t, s) ((t) * 16 + (((s) + KVROT * (t)) & 15))
#define THR_KV 63
#define THR_GU 63
#define THR_WO 63
#define THR_D 63
template <int N> __device__ __forceinline__ void vm_throttle_() { if constexpr (N < 63) asm volatile("s_waitcnt vmcnt(%0)" ::"n"(N) : "memory"); }
#define vm_throttle vm_throttle_
#define QSTG 22
#define STG 8

#define Q_DELAY 4
#define H_DELAY 8
#define ML_DELAY 12
#define O_DELAY 8
#define F_DELAY 10
#define DH_LEFT 10
#define VH_AT 10

extern __shared__ __attribute__((aligned(16))) unsigned char smem[];

__device__ __forceinline__ float lo2f(u32 p) { return __uint_as_float(p << 16); }
__device__ __forceinline__ float hi2f(u32 p) { return __uint_as_float(p & 0xFFFF0000u); }
__device__ __forceinline__ u32 pk(float a, float b) {
  bf16x2 r = {(__bf16)a, (__bf16)b};
  return __builtin_bit_cast(u32, r);
}
__device__ __forceinline__ float rbf(float f) { return lo2f(pk(f, f)); }
__device__ __forceinline__ float sel(bool c, float a, float b) { return c ? a : b; }
__device__ __forceinline__ float sel4(u32 g, float a, float b, float c, float d) {
  const bool b0 = g & 1, b1 = g & 2;
  const float lo = b0 ? b : a, hi = b0 ? d : c;
  return b1 ? hi : lo;
}
__device__ __forceinline__ rsrc_t mk(const void* p, u32 bytes) {
  return __builtin_amdgcn_make_buffer_rsrc((void*)p, 0, bytes, 0x00020000);
}
__device__ __forceinline__ float dot2(u32 w, u32 x, float acc) {
  return __builtin_amdgcn_fdot2_f32_bf16(__builtin_bit_cast(bf16x2, w), __builtin_bit_cast(bf16x2, x), acc, false);
}
template <int CTRL> __device__ __forceinline__ float dpp(float x) {
  return __builtin_bit_cast(float, __builtin_amdgcn_mov_dpp(__builtin_bit_cast(int, x), CTRL, 0xf, 0xf, true));
}
constexpr int DPP_XOR1 = 0xB1, DPP_XOR2 = 0x4E, DPP_HMIRROR = 0x141, DPP_MIRROR = 0x140, DPP_ROR4 = 0x124, DPP_ROR8 = 0x128;
__device__ __forceinline__ float swap16_add(float a, float b) {
  auto s = __builtin_amdgcn_permlane16_swap(__float_as_uint(a), __float_as_uint(b), false, false);
  return __uint_as_float(s[0]) + __uint_as_float(s[1]);
}
__device__ __forceinline__ float swap32_add(float a, float b) {
  auto s = __builtin_amdgcn_permlane32_swap(__float_as_uint(a), __float_as_uint(b), false, false);
  return __uint_as_float(s[0]) + __uint_as_float(s[1]);
}
__device__ __forceinline__ float swap16_max(float a) {
  auto s = __builtin_amdgcn_permlane16_swap(__float_as_uint(a), __float_as_uint(a), false, false);
  return fmaxf(__uint_as_float(s[0]), __uint_as_float(s[1]));
}
__device__ __forceinline__ float swap32_max(float a) {
  auto s = __builtin_amdgcn_permlane32_swap(__float_as_uint(a), __float_as_uint(a), false, false);
  return fmaxf(__uint_as_float(s[0]), __uint_as_float(s[1]));
}
__device__ __forceinline__ float wave_sum(float x) {
  x += dpp<DPP_XOR1>(x); x += dpp<DPP_XOR2>(x); x += dpp<DPP_HMIRROR>(x); x += dpp<DPP_MIRROR>(x);
  x = swap16_add(x, x); x = swap32_add(x, x);
  return x;
}
__device__ __forceinline__ float wave_max(float x) {
  x = fmaxf(x, dpp<DPP_XOR1>(x)); x = fmaxf(x, dpp<DPP_XOR2>(x)); x = fmaxf(x, dpp<DPP_HMIRROR>(x)); x = fmaxf(x, dpp<DPP_MIRROR>(x));
  x = swap16_max(x); x = swap32_max(x);
  return x;
}

struct Tag { u32 t0, t1, t2, t3; };
__device__ __forceinline__ u32 q22(float v, u32 t) { return ((__float_as_uint(v) + 0x200u) & 0xFFFFFC00u) | t; }
__device__ __forceinline__ float r22(float v) { return __uint_as_float((__float_as_uint(v) + 0x200u) & 0xFFFFFC00u); }
__device__ __forceinline__ v4u quad(float a, float b, float c, float d, Tag t) { return v4u{q22(a, t.t0), q22(b, t.t1), q22(c, t.t2), q22(d, t.t3)}; }
__device__ __forceinline__ float gv(u32 d) { return __uint_as_float(d & 0xFFFFFC00u); }
__device__ __forceinline__ f32x4 gv4(v4u v) { return f32x4{gv(v.x), gv(v.y), gv(v.z), gv(v.w)}; }
struct Chk {
  u32 a = 0;
  __device__ __forceinline__ void add(v4u v, Tag t) {
    a = __builtin_amdgcn_bitop3_b32(v.x, t.t0, a, 0xBE); a = __builtin_amdgcn_bitop3_b32(v.y, t.t1, a, 0xBE);
    a = __builtin_amdgcn_bitop3_b32(v.z, t.t2, a, 0xBE); a = __builtin_amdgcn_bitop3_b32(v.w, t.t3, a, 0xBE);
  }
  __device__ __forceinline__ bool ok() const { return __all((a & 0x3FFu) == 0u); }
};
__device__ __forceinline__ void st_quad(rsrc_t rs, u32 off, v4u q) { __builtin_amdgcn_raw_buffer_store_b128(q, rs, off, 0, AUX_SC1); }
__device__ __forceinline__ v4u ld_quad(rsrc_t rs, u32 off) { return __builtin_amdgcn_raw_buffer_load_b128(rs, off, 0, AUX_SC1); }
template <int N, int STG_, class Off, class Use>
__device__ __forceinline__ void gather2(rsrc_t rws, Tag tag, Off off, Use use, u64* cnt) {
  v4u a[N], b[N];
#pragma unroll
  for (int i = 0; i < N; ++i) a[i] = ld_quad(rws, off(i));
  __builtin_amdgcn_s_sleep(STG_);
#pragma unroll 1
  for (u32 sp = 0; sp < SPIN_CAP; ++sp) {
    asm volatile("" ::: "memory");
#pragma unroll
    for (int i = 0; i < N; ++i) b[i] = ld_quad(rws, off(i));
    Chk c;
#pragma unroll
    for (int i = 0; i < N; ++i) c.add(a[i], tag);
    if (cnt) *cnt += 100;
    if (c.ok() || sp + 1 == SPIN_CAP) {
      use(a);
#pragma unroll
      for (int i = 0; i < N; ++i) asm volatile("" ::"v"(b[i]));
      break;
    }
    asm volatile("" ::: "memory");
#pragma unroll
    for (int i = 0; i < N; ++i) a[i] = ld_quad(rws, off(i));
    Chk d;
#pragma unroll
    for (int i = 0; i < N; ++i) d.add(b[i], tag);
    if (cnt) *cnt += 100;
    if (d.ok()) {
      use(b);
#pragma unroll
      for (int i = 0; i < N; ++i) asm volatile("" ::"v"(a[i]));
      break;
    }
  }
}
template <int N, class Off, class Use>
__device__ __forceinline__ void gather1(rsrc_t rws, Tag tag, Off off, Use use, u64* cnt) {
  v4u a[N];
#pragma unroll 1
  for (u32 sp = 0; sp < SPIN_CAP; ++sp) {
    asm volatile("" ::: "memory");
#pragma unroll
    for (int i = 0; i < N; ++i) a[i] = ld_quad(rws, off(i));
    Chk c;
#pragma unroll
    for (int i = 0; i < N; ++i) c.add(a[i], tag);
    if (cnt) *cnt += 100;
    if (c.ok()) break;
  }
  use(a);
}
template <bool PIPED, int N, class Off, class Use>
__device__ __forceinline__ void gather(rsrc_t rws, Tag tag, Off off, Use use, u64* cnt) {
  if constexpr (PIPED) gather2<N, STG>(rws, tag, off, use, cnt); else gather1<N>(rws, tag, off, use, cnt);
}
#define CNT ((u64*)nullptr)
#define GATHER(g, N, OFFEXPR)                                              \
  _Pragma("unroll 1") for (u32 sp_ = 0; sp_ < SPIN_CAP; ++sp_) {          \
    asm volatile("" ::: "memory");                                         \
    Chk c_;                                                                \
    _Pragma("unroll") for (int i = 0; i < N; ++i) g[i] = ld_quad(rws, OFFEXPR); \
    _Pragma("unroll") for (int i = 0; i < N; ++i) c_.add(g[i], tag);      \
    COUNT_ATTEMPT();                                                       \
    if (c_.ok()) break;                                                    \
  }

__device__ __forceinline__ volatile LAS u32* flg(int i) { return (volatile LAS u32*)(smem + L_FLG) + i; }
__device__ __forceinline__ void wait_flag(int i, u32 want) {
  volatile LAS u32* p = flg(i);
#pragma unroll 1
  for (u32 s = 0; s < SPIN_CAP; ++s) {
    if (__builtin_amdgcn_readfirstlane(*p) >= want) break;
    __builtin_amdgcn_s_sleep(1);
  }
  asm volatile("" ::: "memory");
}
__device__ __forceinline__ void wait_flag_tight(int i, u32 want) {
  volatile LAS u32* p = flg(i);
#pragma unroll 1
  for (u32 s = 0; s < SPIN_CAP; ++s) {
    if (__builtin_amdgcn_readfirstlane(*p) >= want) break;
  }
  asm volatile("" ::: "memory");
}
__device__ __forceinline__ Tag gettag() {
  wait_flag(FL_TAG, 1);
  const v4u tg = *(const LAS v4u*)(smem + L_TAG);
  Tag t{(u32)__builtin_amdgcn_readfirstlane(tg.x), (u32)__builtin_amdgcn_readfirstlane(tg.y), (u32)__builtin_amdgcn_readfirstlane(tg.z), (u32)__builtin_amdgcn_readfirstlane(tg.w)};
  asm volatile("" : "+s"(t.t0), "+s"(t.t1), "+s"(t.t2), "+s"(t.t3));
  return t;
}
__device__ __forceinline__ void set_flag(int i) {
  asm volatile("s_waitcnt lgkmcnt(0)" ::: "memory");
  *flg(i) = 1u;
  asm volatile("" ::: "memory");
}
__device__ __forceinline__ void add_flag(int i) {
  asm volatile("s_waitcnt lgkmcnt(0)" ::: "memory");
  if ((threadIdx.x & 63) == 0) __hip_atomic_fetch_add((LAS u32*)(smem + L_FLG) + i, 1u, __ATOMIC_RELAXED, __HIP_MEMORY_SCOPE_WORKGROUP);
  asm volatile("" ::: "memory");
}
__device__ __forceinline__ u32 bump_flag(int i) {
  asm volatile("s_waitcnt lgkmcnt(0)" ::: "memory");
  u32 r = 0;
  if ((threadIdx.x & 63) == 0) r = __hip_atomic_fetch_add((LAS u32*)(smem + L_FLG) + i, 1u, __ATOMIC_RELAXED, __HIP_MEMORY_SCOPE_WORKGROUP);
  r = __builtin_amdgcn_readfirstlane(r);
  asm volatile("" ::: "memory");
  return r;
}
__device__ __forceinline__ void dma16(rsrc_t rs, u32 voff, u32 soff, u32 ldsoff) {
  asm volatile("s_mov_b32 m0, %0\n\ts_nop 0\n\tbuffer_load_dwordx4 %1, %2, %3 offen " DMAPOL " lds" ::"s"(ldsoff), "v"(voff), "s"(rs), "s"(soff) : "m0", "memory");
}
#define G2POL "nt"
__device__ __forceinline__ void dma16b(rsrc_t rs, u32 voff, u32 soff, u32 ldsoff) {
  asm volatile("s_mov_b32 m0, %0\n\ts_nop 0\n\tbuffer_load_dwordx4 %1, %2, %3 offen " G2POL " lds" ::"s"(ldsoff), "v"(voff), "s"(rs), "s"(soff) : "m0", "memory");
}
#define COUNT_ATTEMPT() do { } while (0)
#define VMCNT_(n) asm volatile("s_waitcnt vmcnt(" #n ")" ::: "memory")
#define VMCNT(n) VMCNT_(n)
#define SB() __builtin_amdgcn_sched_barrier(0)
#define BARRIER() do { __builtin_amdgcn_sched_barrier(0); asm volatile("s_waitcnt lgkmcnt(0)" ::: "memory"); __builtin_amdgcn_s_barrier(); asm volatile("" ::: "memory"); __builtin_amdgcn_sched_barrier(0); } while (0)

#define STAMP(i) do { } while (0)
#define STAMP_FLUSH() do { } while (0)

struct Args {
  const bf16_t* x; const bf16_t* kc; const bf16_t* vc; const bf16_t* wq; const bf16_t* wo;
  const bf16_t* wg; const bf16_t* wu; const bf16_t* wd; bf16_t* out; unsigned char* ws;
};

__device__ __forceinline__ float silu_b(float v) {
  return rbf(v * __builtin_amdgcn_rcpf(1.f + __builtin_amdgcn_exp2f(-1.4426950408889634f * v)));
}
__device__ __forceinline__ v4u quad_of_lanes(float v, Tag tag) {
  return quad(dpp<0x00>(v), dpp<0x55>(v), dpp<0xAA>(v), dpp<0xFF>(v), tag);
}

__device__ __forceinline__ void down_gemv(int part, int j, int lane, Tag tag, rsrc_t rws) {
  const int r = j >> 2, c4 = j & 3;
  const u32 lr = lane >> 4, q4 = (lane & 15) >> 2, p4 = lane & 3;
  const u32 badr = L_K + (8 * lr + q4) * 288 + p4 * 8 + part * 96;
  const u32 row4 = 64 + 4 * lr + q4;
  const u32 badr4 = L_K + (row4 < 72 ? row4 : 71) * 288 + p4 * 8 + part * 96;
  const u32 cq = c4 * 36 + (3 * part + lr) * 4 + q4;
  u32 off = (lr < 3 && p4 == 0) ? WS_FIN + (((cq >> 1) * 2 + 1) * 64 + r) * 32 + (cq & 1) * 16 : OOB;
  asm volatile("" : "+v"(off));
  wait_flag(FL_DL, 1);
  s16x4 b[3][5];
#pragma unroll
  for (int t = 0; t < 3; ++t) {
#pragma unroll
    for (int u = 0; u < 4; ++u) b[t][u] = __builtin_amdgcn_ds_read_tr16_b64_v4i16((LAS s16x4*)(smem + badr + ((u >> 1) * 32 + (u & 1) * 4) * 288 + t * 32));
    b[t][4] = __builtin_amdgcn_ds_read_tr16_b64_v4i16((LAS s16x4*)(smem + badr4 + t * 32));
  }
#pragma unroll
  for (int t = 0; t < 3; ++t)
#pragma unroll
    for (int u = 0; u < 5; ++u) asm volatile("" : "+v"(b[t][u]));
  wait_flag_tight(FL_H, 2);
  const s16x8 a0 = *(const LAS s16x8*)(smem + L_HI + lr * 16), a1 = *(const LAS s16x8*)(smem + L_HI + 64 + lr * 16);
  const s16x4 a2 = *(const LAS s16x4*)(smem + L_HI + 128 + lr * 8);
  f32x4 acc[3];
#pragma unroll
  for (int t = 0; t < 3; ++t) {
    f32x4 c = {0.f, 0.f, 0.f, 0.f};
    c = __builtin_amdgcn_mfma_f32_16x16x16bf16_1k(a2, b[t][4], c, 0, 0, 0);
    c = __builtin_amdgcn_mfma_f32_16x16x32_bf16(__builtin_bit_cast(bf16x8, a0), __builtin_bit_cast(bf16x8, __builtin_shufflevector(b[t][0], b[t][1], 0, 1, 2, 3, 4, 5, 6, 7)), c, 0, 0, 0);
    c = __builtin_amdgcn_mfma_f32_16x16x32_bf16(__builtin_bit_cast(bf16x8, a1), __builtin_bit_cast(bf16x8, __builtin_shufflevector(b[t][2], b[t][3], 0, 1, 2, 3, 4, 5, 6, 7)), c, 0, 0, 0);
    acc[t] = c;
  }
  st_quad(rws, off, quad_of_lanes(sel4(lr, acc[0][0], acc[1][0], acc[2][0], acc[2][0]), tag));
  add_flag(FL_FIN);
}

__global__ void __launch_bounds__(NTHR, 3) fused_kernel(Args a) {
  const int tid = threadIdx.x, lane = tid & 63;
  const int wave = __builtin_amdgcn_readfirstlane(tid >> 6);
  const int b = blockIdx.x;
  const int j = (b & 7) * 32 + (b >> 3);
  STAMP(0);
  if (wave == 11) {
    if (lane < 32) *flg(lane) = 0u;
    if (lane >= 32 && lane < 36) *(LAS u32*)(smem + L_HI + 144 + (lane - 32) * 4) = 0u;
  }
  BARRIER();

  if (__builtin_expect(wave < 4, 1)) {
    __builtin_amdgcn_s_setprio(3);
    const int kp = j & 3, r = j >> 2;
    const bf16_t* wm; u32 row0, stride, strip, nrows, pub;
    if (wave == 0) { wm = a.wq; row0 = 144 * kp; stride = NQ * 2; strip = r; nrows = 144; pub = WS_Q + j * 256; }
    else if (wave < 3) {
      const int quarter = 2 * (kp & 1) + (wave - 1);
      wm = (kp >> 1) ? a.wu : a.wg; row0 = 144 * quarter; stride = F * 2; nrows = 144;
      strip = (r & ~7) | ((r + 3 * wave) & 7);
      pub = WS_GM + ((strip * 2 + (kp >> 1)) * 4 + quarter) * 256;
    } else { wm = ((j >> 4) & 1) ? a.wu : a.wg; row0 = 36 * (j & 15); stride = F * 2; strip = 64 + (j >> 5); nrows = 36; pub = WS_GP + j * 256; }
    const rsrc_t rw = mk((const unsigned char*)wm + (size_t)row0 * stride + strip * 128, (nrows - 1) * stride + 128);
    const rsrc_t rx = mk(a.x, D * 2);
    const rsrc_t rws = mk(a.ws, WS_END);
    const u64 ep = __hip_atomic_load((GAS u64*)(a.ws + WS_EPOCH), __ATOMIC_RELAXED, __HIP_MEMORY_SCOPE_AGENT);
    u32 xr[5];
#pragma unroll
    for (int i = 0; i < 5; ++i) xr[i] = __builtin_amdgcn_raw_buffer_load_b32(rx, lane * 4, i * 256, 0);
    const u32 rs8 = lane >> 3;
    const u32 voff = rs8 * stride + (lane & 7) * 16;
    u64 tg = ep;
    Tag tag;
    auto layernorm = [&](v4u (&av)[18]) {
#pragma unroll
      for (int i = 0; i < 5; ++i) asm volatile("" : "+v"(xr[i]));
      asm volatile("" : "+v"(tg));
      tg += 1;
      {
        const u64 T = ((u64)(u32)__builtin_amdgcn_readfirstlane((u32)(tg >> 32)) << 32) | (u32)__builtin_amdgcn_readfirstlane((u32)tg);
        const u64 H = T * 0x9E3779B97F4A7C15ull;
        tag = Tag{(u32)T & 0x3FFu, (u32)(H >> 54), (u32)(H >> 44) & 0x3FFu, (u32)(H >> 34) & 0x3FFu};
        if (wave == 0) {
          if (lane == 0) { *(LAS v4u*)(smem + L_TAG) = v4u{tag.t0, tag.t1, tag.t2, tag.t3}; *(LAS v2u*)(smem + L_TAG + 16) = v2u{(u32)T, (u32)(T >> 32)}; }
          set_flag(FL_TAG);
        }
      }
      float s = 0.f, q = 0.f;
#pragma unroll
      for (int i = 0; i < 5; ++i) { s = dot2(xr[i], 0x3F803F80u, s); q = dot2(xr[i], xr[i], q); }
      s = wave_sum(s); q = wave_sum(q);
      const float mean = s * (1.f / 576.f);
      const float var = rbf(fmaxf(fmaf(-mean, mean, q * (1.f / 576.f)), 0.f));
      const float mb = rbf(mean);
      const float inv = __builtin_amdgcn_rcpf(rbf(__builtin_amdgcn_sqrtf(rbf(var + 1e-5f))));
      LAS v2u* xn = (LAS v2u*)(smem + L_XN);
#pragma unroll
      for (int i = 0; i < 5; ++i) {
        const u32 d = pk(lo2f(xr[i]) - mb, hi2f(xr[i]) - mb);
        const u32 n = pk(lo2f(d) * inv, hi2f(d) * inv);
        if (i < 4 || lane < 32) xn[i * 64 + lane] = v2u{__builtin_amdgcn_perm(n, n, 0x01000100u), __builtin_amdgcn_perm(n, n, 0x03020302u)};
      }
      asm volatile("s_waitcnt lgkmcnt(0)" ::: "memory");
      u32 xp[18];
      const LAS u32* xs = (const LAS u32*)(smem + L_XN) + row0 + rs8;
#pragma unroll
      for (int i = 0; i < 18; ++i) xp[i] = xs[(wave == 3 && i > 4) ? 0 : 8 * i];
      if (wave == 3 && rs8 >= 4) xp[4] = 0u;
      u32 msk[4];
#pragma unroll
      for (int d = 0; d < 4; ++d) msk[d] = (((lane & 7) >> 1) == d) ? ((lane & 1) ? 0xFFFF0000u : 0x0000FFFFu) : 0u;
#pragma unroll
      for (int i = 0; i < 18; ++i) av[i] = v4u{xp[i] & msk[0], xp[i] & msk[1], xp[i] & msk[2], xp[i] & msk[3]};
    };
    auto gemv_publish = [&](const v4u (&av)[18], const v4u (&w)[18]) {
      f32x4 da = {0.f, 0.f, 0.f, 0.f}, db = {0.f, 0.f, 0.f, 0.f};
#pragma unroll
      for (int i = 0; i < 18; ++i) {
        if (i & 1) db = __builtin_amdgcn_mfma_f32_16x16x32_bf16(__builtin_bit_cast(bf16x8, av[i]), __builtin_bit_cast(bf16x8, w[i]), db, 0, 0, 0);
        else da = __builtin_amdgcn_mfma_f32_16x16x32_bf16(__builtin_bit_cast(bf16x8, av[i]), __builtin_bit_cast(bf16x8, w[i]), da, 0, 0, 0);
      }
      da += db;
      f32x4 res;
#pragma unroll
      for (int k = 0; k < 4; ++k) {
        const float up = dpp<DPP_ROR8>(da[k]);
        res[k] = da[k] + __uint_as_float(__builtin_amdgcn_permlane32_swap(__float_as_uint(up), __float_as_uint(up), false, false)[1]);
      }
      const u32 off = ((lane & 0x28) == 0) ? pub + (2 * (lane & 7) + (lane >> 4)) * 16 : OOB;
      st_quad(rws, off, quad(res[0], res[1], res[2], res[3], tag));
    };
    if (__builtin_expect(wave == 0, 1)) {
      v4u w[18], av[18];
#pragma unroll
      for (int i = 0; i < 18; ++i) w[i] = __builtin_amdgcn_raw_buffer_load_b128(rw, voff, 8 * i * stride, AUX_NT);
      SB();
      set_flag(FL_W0);
      STAMP(1);
      layernorm(av);
      STAMP(2);
      gemv_publish(av, w);
      set_flag(FL_QPUB);
      STAMP(3);
      {
        const u32 bm = WS_GM + r * 2048 + lane * 16;
        wait_flag(FL_HPUB, 3);
        __builtin_amdgcn_s_sleep(H_DELAY);
        wait_flag(FL_DH, 1);
        STAMP(4);
        gather<(PIPE & 1) != 0, 2>(rws, tag, [&](int i) { return bm + i * 1024; }, [&](const v4u (&g)[2]) {
          STAMP(5);
          const f32x4 gg = gv4(g[0]), uu = gv4(g[1]);
          const float gs = swap16_add(swap32_add(gg[0], gg[2]), swap32_add(gg[1], gg[3]));
          const float us = swap16_add(swap32_add(uu[0], uu[2]), swap32_add(uu[1], uu[3]));
          const float hv = silu_b(rbf(gs)) * rbf(us);
          ((LAS bf16_t*)(smem + L_HI))[(lane & 15) * 4 + (lane >> 4)] = (bf16_t)(pk(hv, hv) & 0xFFFFu);
          add_flag(FL_H);
        }, CNT);
        STAMP(6);
      }
      if ((j & 31) < 9) {
        const int f = (j >> 5) * 9 + (j & 31);
        const u32 mycol = f * 8 + 4 * (lane & 1) + 2 * ((lane >> 4) & 1) + ((lane >> 3) & 1);
        const u32 xo = __builtin_amdgcn_raw_buffer_load_b16(rx, mycol * 2, 0, 0);
        const u32 base = WS_FIN + f * 4096 + lane * 16;
        wait_flag(FL_FIN, 5);
        __builtin_amdgcn_s_sleep(F_DELAY);
        STAMP(7);
        gather<(PIPE & 4) != 0, 4>(rws, tag, [&](int i) { return base + i * 1024; }, [&](const v4u (&g)[4]) {
        STAMP(1);
        const f32x4 sv = gv4(g[0]) + gv4(g[1]), fv = gv4(g[2]) + gv4(g[3]);
        float a4[4];
#pragma unroll
        for (int k = 0; k < 4; ++k) a4[k] = swap32_add(sv[k], fv[k]);
        const float a0 = swap16_add(a4[0], a4[2]), a1 = swap16_add(a4[1], a4[3]);
        float b0 = a0 + dpp<DPP_XOR2>(a0), b1 = a1 + dpp<DPP_XOR2>(a1);
        b0 += dpp<DPP_ROR4>(b0); b1 += dpp<DPP_ROR4>(b1);
        b0 += dpp<DPP_ROR8>(b0); b1 += dpp<DPP_ROR8>(b1);
        const float t = (lane & 8) ? b1 : b0;
        const auto sf = __builtin_amdgcn_permlane32_swap(__float_as_uint(t), __float_as_uint(t), false, false);
        const float sb = rbf(__uint_as_float(sf[0])), fb = rbf(__uint_as_float(sf[1]));
        const float o = lo2f(xo) + rbf(sb * silu_b(fb));
        if ((lane & 0x26) == 0) *(GAS bf16_t*)((GAS unsigned char*)a.out + mycol * 2) = (bf16_t)(pk(o, o) & 0xFFFFu);
        }, CNT);
        if (f == 0 && lane == 0) { const v2u nx = *(const LAS v2u*)(smem + L_TAG + 16); __hip_atomic_store((GAS u64*)(a.ws + WS_EPOCH), (u64)nx.x | ((u64)nx.y << 32), __ATOMIC_RELAXED, __HIP_MEMORY_SCOPE_AGENT); }
        STAMP(2);
      }
    } else {
      v4u w[18], av[18];
      layernorm(av);
      STAMP(2);
      __builtin_amdgcn_s_setprio(1);
#pragma unroll
      for (int i = 0; i < 18; ++i) asm volatile("" : "+v"(av[i]));
      wait_flag(FL_VISS, 4);
#pragma unroll
      for (int i = 0; i < 18; ++i) { vm_throttle<THR_GU>(); SB(); w[i] = __builtin_amdgcn_raw_buffer_load_b128(rw, voff, 8 * i * stride, AUX_NT); SB(); }
      SB();
      add_flag(FL_G0);
      __builtin_amdgcn_s_setprio(3);
      STAMP(5);
      gemv_publish(av, w);
      add_flag(FL_HPUB);
      STAMP(3);
      if (wave == 1) {
        const u32 bp = WS_GP + ((r >> 3) * 32 + (lane >> 1)) * 256 + ((r & 7) * 2 + (lane & 1)) * 16;
        wait_flag(FL_HPUB, 3);
        __builtin_amdgcn_s_sleep(H_DELAY);
        wait_flag(FL_DH, 1);
        gather<(PIPE & 1) != 0, 1>(rws, tag, [&](int) { return bp; }, [&](const v4u (&g)[1]) {
        STAMP(4);
        f32x4 t = gv4(g[0]);
#pragma unroll
        for (int k = 0; k < 4; ++k) t[k] += dpp<DPP_XOR2>(t[k]);
        float a0 = swap16_add(t[0], t[2]), a1 = swap16_add(t[1], t[3]);
        a0 += dpp<DPP_ROR4>(a0); a1 += dpp<DPP_ROR4>(a1);
        a0 += dpp<DPP_ROR8>(a0); a1 += dpp<DPP_ROR8>(a1);
        const float v = (lane & 8) ? a1 : a0;
        const auto gu = __builtin_amdgcn_permlane32_swap(__float_as_uint(v), __float_as_uint(v), false, false);
        const float hv = silu_b(rbf(__uint_as_float(gu[0]))) * rbf(__uint_as_float(gu[1]));
        if ((lane & 0x26) == 0) ((LAS bf16_t*)(smem + L_HI))[64 + 4 * (lane & 1) + 2 * ((lane >> 4) & 1) + ((lane >> 3) & 1)] = (bf16_t)(pk(hv, hv) & 0xFFFFu);
        add_flag(FL_H);
        }, CNT);
        STAMP(6);
      }
      if (wave >= 2) {
        down_gemv(wave - 1, j, lane, tag, rws);
        STAMP(6);
      }
    }
  } else if (__builtin_expect(wave < 8, 1)) {
    __builtin_amdgcn_s_setprio(0);
    const int w = wave - 4, g = j >> 4, s = j & 15;
    const rsrc_t rk = mk(a.kc + ((size_t)g * SK + (SK - WIN)) * DH, WIN * DH * 2);
    const rsrc_t rv = mk(a.vc + ((size_t)g * SK + (SK - WIN)) * DH, WIN * DH * 2);
    const rsrc_t rws = mk(a.ws, WS_END);
    const u32 lr = lane >> 4;
    u32 vo[4];
#pragma unroll
    for (int sub = 0; sub < 4; ++sub) { vo[sub] = sub * 1024 + lr * 256 + ((((lane & 15) ^ (lr << 2)) ^ sub) << 4); asm volatile("" : "+v"(vo[sub])); }
    __builtin_amdgcn_s_sleep(K_GATE);
#pragma unroll
    for (int p = 0; p < 16; ++p) { vm_throttle<THR_KV>(); dma16(rk, vo[p & 3], KVTILE(4 * w + (p >> 2), s) * 4096, L_K + w * 16384 + p * 1024); }
#pragma unroll
    for (int p = 0; p < 16; ++p) {
      vm_throttle<THR_KV>();
      dma16(rv, vo[p & 3], KVTILE(4 * w + (p >> 2), s) * 4096, L_V + w * 16384 + p * 1024);
      if (p == VH_AT && w == 0) *flg(FL_VH) = 1u;
    }
    add_flag(FL_VISS);
    __builtin_amdgcn_s_setprio(3);
    STAMP(1);
    const Tag tag = gettag();
    const u32 krow = lane & 15;
    u32 kadr = L_K + w * 16384 + krow * 256 + ((lr ^ (((krow & 3) << 2) | ((krow >> 2) & 3))) << 4);
    const u32 q4 = (lane & 15) >> 2, p4 = lane & 3;
    u32 va[2][8];
#pragma unroll
    for (int t = 0; t < 2; ++t) {
      const u32 row = 8 * (lr & 1) + 4 * t + q4;
      const u32 base = L_V + w * 16384 + (lr >> 1) * 4096 + row * 256 + (((p4 >> 1) ^ ((q4 << 2) | (2 * (lr & 1) + t))) << 4) + 8 * (p4 & 1);
#pragma unroll
      for (int c = 0; c < 8; ++c) { va[t][c] = base ^ (c << 5); asm volatile("" : "+v"(va[t][c])); }
    }
    u32 ob = WS_OP + (((2 * g + (lane >> 5)) * 16 + s) * 32 + (lane & 31)) * 16;
    asm volatile("" : "+v"(kadr), "+v"(ob));
    VMCNT(16);
    s16x8 kf[16];
#pragma unroll
    for (int i = 0; i < 4; ++i)
#pragma unroll
      for (int st = 0; st < 4; ++st) kf[i * 4 + st] = *(const LAS s16x8*)(smem + ((kadr ^ (st << 6)) + i * 4096));
#pragma unroll
    for (int i = 0; i < 16; ++i) asm volatile("" : "+v"(kf[i]));
    add_flag(FL_MX);
    STAMP(2);
    wait_flag(FL_Q, 1);
    STAMP(3);
    s16x8 qf[4];
#pragma unroll
    for (int st = 0; st < 4; ++st) qf[st] = *(const LAS s16x8*)(smem + L_QI + (lane & 1) * 256 + st * 64 + lr * 16);
    f32x4 sc[4];
#pragma unroll
    for (int i = 0; i < 4; ++i) sc[i] = f32x4{0.f, 0.f, 0.f, 0.f};
#pragma unroll
    for (int st = 0; st < 4; ++st)
#pragma unroll
      for (int i = 0; i < 4; ++i)
        sc[i] = __builtin_amdgcn_mfma_f32_16x16x32_bf16(__builtin_bit_cast(bf16x8, qf[st]), __builtin_bit_cast(bf16x8, kf[i * 4 + st]), sc[i], 0, 0, 0);
    constexpr float C2 = 0.08838834764831845f * 1.4426950408889634f;
    const float s0 = sel4(lr, sc[0][0], sc[1][0], sc[2][0], sc[3][0]) * C2;
    const float s1 = sel4(lr, sc[0][1], sc[1][1], sc[2][1], sc[3][1]) * C2;
    LAS float* mx = (LAS float*)(smem + L_MX);
    {
      const float wm0 = wave_max(s0), wm1 = wave_max(s1);
      if (lane == 0) *(LAS v2u*)(mx + 2 * w) = v2u{__float_as_uint(wm0), __float_as_uint(wm1)};
    }
    add_flag(FL_M2);
    wait_flag_tight(FL_M2, 4);
    const f32x4 ma = *(const LAS f32x4*)(mx), mb4 = *(const LAS f32x4*)(mx + 4);
    const float m0 = r22(fmaxf(fmaxf(ma[0], ma[2]), fmaxf(mb4[0], mb4[2])));
    const float m1 = r22(fmaxf(fmaxf(ma[1], ma[3]), fmaxf(mb4[1], mb4[3])));
    const u32 pp = pk(__builtin_amdgcn_exp2f(s0 - m0), __builtin_amdgcn_exp2f(s1 - m1));
    LAS bf16_t* pi = (LAS bf16_t*)(smem + L_PI + w * 256);
    pi[lane] = (bf16_t)(pp & 0xFFFFu); pi[64 + lane] = (bf16_t)(pp >> 16);
    {
      const float l0 = wave_sum(lo2f(pp)), l1 = wave_sum(hi2f(pp));
      if (lane == 0) *(LAS v2u*)(mx + 8 + 2 * w) = v2u{__float_as_uint(l0), __float_as_uint(l1)};
    }
    s16x8 pf[2];
#pragma unroll
    for (int ks = 0; ks < 2; ++ks) pf[ks] = *(const LAS s16x8*)(smem + L_PI + w * 256 + (lane & 1) * 128 + ks * 64 + lr * 16);
    f32x4 oc[8];
#pragma unroll
    for (int c = 0; c < 8; ++c) oc[c] = f32x4{0.f, 0.f, 0.f, 0.f};
    STAMP(4);
#pragma unroll
    for (int ks = 0; ks < 2; ++ks) {
      if (ks == 0) VMCNT(8); else VMCNT(0);
      if (ks == 1) STAMP(5);
      s16x4 b0[8], b1[8];
#pragma unroll
      for (int c = 0; c < 8; ++c) {
        b0[c] = __builtin_amdgcn_ds_read_tr16_b64_v4i16((LAS s16x4*)(smem + va[0][c] + ks * 8192));
        b1[c] = __builtin_amdgcn_ds_read_tr16_b64_v4i16((LAS s16x4*)(smem + va[1][c] + ks * 8192));
      }
      SB();
#pragma unroll
      for (int c = 0; c < 8; ++c) {
        const s16x8 bb = __builtin_shufflevector(b0[c], b1[c], 0, 1, 2, 3, 4, 5, 6, 7);
        oc[c] = __builtin_amdgcn_mfma_f32_16x16x32_bf16(__builtin_bit_cast(bf16x8, pf[ks]), __builtin_bit_cast(bf16x8, bb), oc[c], 0, 0, 0);
      }
      SB();
    }
    if (lane < 16) {
      LAS float* ow = (LAS float*)(smem + L_OW) + w * 256 + lane;
#pragma unroll
      for (int c = 0; c < 8; ++c) { ow[c * 16] = oc[c][0]; ow[128 + c * 16] = oc[c][1]; }
    }
    const u32 arrived = bump_flag(FL_OW);
    STAMP(6);
    if (arrived == 3) {
      f32x4 t[4];
#pragma unroll
      for (int u = 0; u < 4; ++u) t[u] = *(const LAS f32x4*)(smem + L_OW + u * 1024 + lane * 16);
      const f32x4 la = *(const LAS f32x4*)(mx + 8), lb = *(const LAS f32x4*)(mx + 12);
      const f32x4 o = (t[0] + t[1]) + (t[2] + t[3]);
      st_quad(rws, ob, quad(o[0], o[1], o[2], o[3], tag));
      const float L = (lane >> 5) ? (la[1] + la[3]) + (lb[1] + lb[3]) : (la[0] + la[2]) + (lb[0] + lb[2]);
      st_quad(rws, (lane & 31) == 0 ? WS_ML + ((2 * g + (lane >> 5)) * 16 + s) * 16 : OOB, quad((lane >> 5) ? m1 : m0, L, 0.f, 0.f, tag));
      set_flag(FL_OPUB);
      STAMP(7);
    }
  } else if (wave < 10) {
    __builtin_amdgcn_s_setprio(1);
    v4u wpf[4];
    if (wave == 9) {
      constexpr u32 SL = ((WS_END - WS_Q + 255) / 256 + 1023) / 1024 * 1024;
#pragma unroll
      for (int i = 0; i < 4; ++i) wpf[i] = __builtin_amdgcn_raw_buffer_load_b128(mk(a.ws, WS_END), (u32)i * 1024 < SL ? WS_Q + j * SL + i * 1024 + lane * 16 : OOB, 0, WSPF);
    }
    u32 pf_dummy = 0;
    if (wave == 8) {
      const int k = j & 31, x = j >> 5;
      const unsigned char* p = nullptr;
      if (k < 2) p = (const unsigned char*)(a.vc + ((size_t)(2 * x + k) * SK + (SK - WIN)) * DH);
      else if (k < 4) p = (const unsigned char*)(a.kc + ((size_t)(2 * x + k - 2) * SK + (SK - WIN)) * DH);
      else if (k < 7) p = (const unsigned char*)a.wg + (size_t)(k - 4) * (2u << 20) + 1024 * x;
      else if (k < 10) p = (const unsigned char*)a.wu + (size_t)(k - 7) * (2u << 20) + 1024 * x;
      else if (k < 12) p = (const unsigned char*)a.wo + (size_t)(512 * x + 511 * (k - 10)) * 1152;
      else if (k < 14) p = (const unsigned char*)a.wd + (size_t)(512 * x + 511 * (k - 12)) * 1152;
      else if (k == 14) p = (const unsigned char*)a.wd + (size_t)(4096 + 64 * x) * 1152;
      else if (k < 18) p = (const unsigned char*)a.wq + (size_t)(k - 15) * (2u << 20) + 1024 * x;
      else if (k == 18) p = a.ws + WS_FIN + 4096 * x;
      if (p != nullptr) pf_dummy = *(const GAS u32*)p;
    }
    const int w2 = wave & 1;
    const int r = j >> 2, c4 = j & 3;
    const rsrc_t rw = mk(a.wo, NQ * D * 2);
    const rsrc_t rws = mk(a.ws, WS_END);
    u32 vo[9];
#pragma unroll
    for (int i = 0; i < 9; ++i) {
      const u32 id = (w2 + 2 * i) * 64 + lane;
      const u32 row = (id * 3641u) >> 16, c = id - row * 18;
      vo[i] = (64 * r + row) * 1152 + c4 * 288 + c * 16;
      asm volatile("" : "+v"(vo[i]));
    }
    const Tag tag = gettag();
    asm volatile("" : "+v"(pf_dummy));
    if (wave == 9) {
#pragma unroll
      for (int i = 0; i < 4; ++i) asm volatile("" :: "v"(wpf[i]));
    }
    wait_flag(FL_G0, 3);
    STAMP(1);
#pragma unroll
    for (int i = 0; i < 9; ++i) { vm_throttle<THR_WO>(); dma16b(rw, vo[i], 0, L_WO + (w2 + 2 * i) * 1024); }
    __builtin_amdgcn_s_setprio(3);
    STAMP(2);
    VMCNT(0);
    add_flag(FL_WOL);
    wait_flag(FL_WOL, 2);
    STAMP(3);
    const u32 lr = lane >> 4, q4 = (lane & 15) >> 2, p4 = lane & 3;
    const u32 badr = L_WO + (8 * lr + q4) * 288 + p4 * 8 + w2 * 32;
    s16x4 bb[5][4];
#pragma unroll
    for (int t = 0; t < 5; ++t)
#pragma unroll
      for (int u = 0; u < 4; ++u)
        bb[t][u] = __builtin_amdgcn_ds_read_tr16_b64_v4i16((LAS s16x4*)(smem + badr + ((u >> 1) * 32 + (u & 1) * 4) * 288 + ((t == 4 && w2) ? 3 : t) * 64));
    u32 offA, offB;
    {
      const u32 cqA = c4 * 36 + (w2 + 2 * lr) * 4 + q4, cqB = c4 * 36 + 32 + q4;
      offA = p4 == 0 ? WS_FIN + (((cqA >> 1) * 2) * 64 + r) * 32 + (cqA & 1) * 16 : OOB;
      offB = (w2 == 0 && lr == 0 && p4 == 0) ? WS_FIN + (((cqB >> 1) * 2) * 64 + r) * 32 + (cqB & 1) * 16 : OOB;
      asm volatile("" : "+v"(offA), "+v"(offB));
    }
#pragma unroll
    for (int t = 0; t < 5; ++t)
#pragma unroll
      for (int u = 0; u < 4; ++u) asm volatile("" : "+v"(bb[t][u]));
    wait_flag_tight(FL_O, 1);
    STAMP(4);
    const s16x8 a0 = *(const LAS s16x8*)(smem + L_OI + lr * 16), a1 = *(const LAS s16x8*)(smem + L_OI + 64 + lr * 16);
    f32x4 acc[5];
#pragma unroll
    for (int t = 0; t < 5; ++t) {
      f32x4 c = {0.f, 0.f, 0.f, 0.f};
      c = __builtin_amdgcn_mfma_f32_16x16x32_bf16(__builtin_bit_cast(bf16x8, a0), __builtin_bit_cast(bf16x8, __builtin_shufflevector(bb[t][0], bb[t][1], 0, 1, 2, 3, 4, 5, 6, 7)), c, 0, 0, 0);
      c = __builtin_amdgcn_mfma_f32_16x16x32_bf16(__builtin_bit_cast(bf16x8, a1), __builtin_bit_cast(bf16x8, __builtin_shufflevector(bb[t][2], bb[t][3], 0, 1, 2, 3, 4, 5, 6, 7)), c, 0, 0, 0);
      acc[t] = c;
    }
    st_quad(rws, offA, quad_of_lanes(sel4(lr, acc[0][0], acc[1][0], acc[2][0], acc[3][0]), tag));
    if (w2 == 0) st_quad(rws, offB, quad_of_lanes(acc[4][0], tag));
    add_flag(FL_FIN);
    STAMP(5);
  } else if (wave == 10) {
    __builtin_amdgcn_s_setprio(1);
    const int r = j >> 2, c4 = j & 3;
    const rsrc_t rw = mk(a.wd, F * D * 2);
    const rsrc_t rws = mk(a.ws, WS_END);
    u32 vo[21];
#pragma unroll
    for (int i = 0; i < 21; ++i) {
      const u32 id = i * 64 + lane;
      const u32 row = (id * 3641u) >> 16, c = id - row * 18;
      const u32 grow = row >= 64 ? 4096 + 8 * r + (row - 64) : 64 * r + row;
      vo[i] = id < 1296u ? grow * 1152 + c4 * 288 + c * 16 : OOB;
      asm volatile("" : "+v"(vo[i]));
    }
    const Tag tag = gettag();
    wait_flag(FL_G0, 3);
    wait_flag(FL_MX, 4);
    STAMP(1);
#pragma unroll
    for (int i = 0; i < 21; ++i) { vm_throttle<THR_D>(); dma16b(rw, vo[i], 0, L_K + i * 1024); }
    __builtin_amdgcn_s_setprio(3);
    STAMP(2);
    VMCNT(DH_LEFT);
    *flg(FL_DH) = 1u;
    VMCNT(0);
    set_flag(FL_DL);
    STAMP(3);
    down_gemv(0, j, lane, tag, rws);
    STAMP(5);
  } else {
    __builtin_amdgcn_s_setprio(3);
    const rsrc_t rws = mk(a.ws, WS_END);
    const int h = j >> 3, hf = (j >> 2) & 1;
    const Tag tag = gettag();
    const u32 lr = lane >> 4;
    {
      const u32 base = WS_Q + (j & ~15) * 256 + lane * 16;
      wait_flag(FL_QPUB, 1);
      __builtin_amdgcn_s_sleep(Q_DELAY);
      wait_flag(FL_VH, 1);
      STAMP(1);
      auto useq = [&](const v4u (&g)[4]) {
        f32x4 v[4];
#pragma unroll
        for (int i = 0; i < 4; ++i) v[i] = gv4(g[i]);
        float qv[4];
#pragma unroll
        for (int k = 0; k < 4; ++k) qv[k] = swap16_add(swap32_add(v[0][k], v[2][k]), swap32_add(v[1][k], v[3][k]));
        *(LAS v2u*)(smem + L_QI + (lr * 64 + (lane & 15) * 4) * 2) = v2u{pk(qv[0], qv[1]), pk(qv[2], qv[3])};
        set_flag(FL_Q);
      };
      if constexpr ((PIPE & 8) != 0) gather2<4, QSTG>(rws, tag, [&](int i) { return base + i * 1024; }, useq, CNT);
      else gather1<4>(rws, tag, [&](int i) { return base + i * 1024; }, useq, CNT);
      STAMP(2);
    }
    {
      const u32 bo = WS_OP + ((h * 16 + lr) * 32 + hf * 16 + (lane & 15)) * 16;
      const u32 bl = WS_ML + (h * 16 + ((lane & 12) | ((lane + lr) & 3))) * 16;
      wait_flag(FL_OPUB, 1);
      __builtin_amdgcn_s_sleep(O_DELAY);
      wait_flag(FL_DH, 1);
      STAMP(5);
      gather<(PIPE & 2) != 0, 5>(rws, tag, [&](int i) { return i == 0 ? bl : bo + (i - 1) * 2048; }, [&](const v4u (&g)[5]) {
      STAMP(6);
      const float m = gv(g[0].x), l = gv(g[0].y);
      float M = m; M = fmaxf(M, dpp<DPP_XOR1>(M)); M = fmaxf(M, dpp<DPP_XOR2>(M)); M = fmaxf(M, dpp<DPP_HMIRROR>(M)); M = fmaxf(M, dpp<DPP_MIRROR>(M));
      const float wt = __builtin_amdgcn_exp2f(m - M);
      float L = wt * l; L += dpp<DPP_XOR1>(L); L += dpp<DPP_XOR2>(L); L += dpp<DPP_HMIRROR>(L); L += dpp<DPP_MIRROR>(L);
      f32x4 acc = dpp<0x150>(wt) * gv4(g[1]);
      acc += dpp<0x154>(wt) * gv4(g[2]);
      acc += dpp<0x158>(wt) * gv4(g[3]);
      acc += dpp<0x15C>(wt) * gv4(g[4]);
      const float o = swap16_add(swap32_add(acc[0], acc[2]), swap32_add(acc[1], acc[3])) * __builtin_amdgcn_rcpf(L);
      ((LAS bf16_t*)(smem + L_OI))[(lane & 15) * 4 + lr] = (bf16_t)(pk(o, o) & 0xFFFFu);
      set_flag(FL_O);
      }, CNT);
      STAMP(7);
    }
  }
  STAMP(9);
  STAMP_FLUSH();
}

}

extern "C" void kernel_launch(void* const* d_in, const int* in_sizes, int n_in, void* d_out, int out_size, void* d_ws, size_t ws_size,
                              hipStream_t stream) {
  Args a;
  a.x = (const bf16_t*)d_in[0]; a.kc = (const bf16_t*)d_in[1]; a.vc = (const bf16_t*)d_in[2];
  a.wq = (const bf16_t*)d_in[3]; a.wo = (const bf16_t*)d_in[4];
  a.wg = (const bf16_t*)d_in[6]; a.wu = (const bf16_t*)d_in[7]; a.wd = (const bf16_t*)d_in[8];
  a.out = (bf16_t*)d_out; a.ws = (unsigned char*)d_ws;
  (void)hipFuncSetAttribute((const void*)fused_kernel, hipFuncAttributeMaxDynamicSharedMemorySize, L_END);
  fused_kernel<<<256, NTHR, L_END, stream>>>(a);
}
```

```cpp
#include <hip/hip_runtime.h>
#include <stdint.h>

typedef unsigned short bf16_t;
typedef unsigned u32;
typedef unsigned long long u64;
typedef u32 v4u __attribute__((ext_vector_type(4)));
typedef u32 v2u __attribute__((ext_vector_type(2)));
typedef float f32x4 __attribute__((ext_vector_type(4)));
typedef short s16x4 __attribute__((ext_vector_type(4)));
typedef short s16x8 __attribute__((ext_vector_type(8)));
typedef __bf16 bf16x2 __attribute__((ext_vector_type(2)));
typedef __bf16 bf16x8 __attribute__((ext_vector_type(8)));
#define LAS __attribute__((address_space(3)))
#define GAS __attribute__((address_space(1)))
typedef __amdgpu_buffer_rsrc_t rsrc_t;

namespace {

constexpr int D = 576, SK = 65536, DH = 128, WIN = 4096, F = 4608, NQ = 4096;
constexpr int NWAVES = 12, NTHR = NWAVES * 64;

constexpr u32 WS_EPOCH = 0;
constexpr u32 WS_Q = 4096;
constexpr u32 WS_GM = WS_Q + 256 * 256;
constexpr u32 WS_GP = WS_GM + 64 * 8 * 256;
constexpr u32 WS_OP = WS_GP + 256 * 256;
constexpr u32 WS_ML = WS_OP + 32 * 16 * 512;
constexpr u32 WS_FIN = WS_ML + 32 * 16 * 16;
constexpr u32 WS_END = WS_FIN + 72 * 2 * 64 * 32;

constexpr u32 L_K = 0;
constexpr u32 L_V = 65536;
constexpr u32 L_WO = 131072;
constexpr u32 L_XN = L_WO + 18432;
constexpr u32 L_QI = L_XN + 2304;
constexpr u32 L_PI = L_QI + 512;
constexpr u32 L_OW = L_PI + 1024;
constexpr u32 L_HI = L_OW + 4096;
constexpr u32 L_OI = L_HI + 160;
constexpr u32 L_MX = L_OI + 128;
constexpr u32 L_TAG = L_MX + 64;
constexpr u32 L_FLG = L_TAG + 32;
constexpr u32 L_END = L_FLG + 128;
static_assert(L_END <= 163840, "LDS");
enum { FL_Q, FL_VISS, FL_MX, FL_OW, FL_OPUB, FL_H, FL_O, FL_WOL, FL_FIN, FL_G0, FL_TAG, FL_W0, FL_M2, FL_QPUB, FL_HPUB, FL_DL, FL_VH, FL_DH };

constexpr int AUX_NT = 2, AUX_SC1 = 16;
constexpr u32 SPIN_CAP = 1u << 20;
constexpr u32 OOB = 0x7FFFFFF0u;

#define Q_DELAY 4
#define H_DELAY 8
#define O_DELAY 8
#define F_DELAY 8
#define VH_AT 10
#define DH_LEFT 7
#define KVTILE(t, s) ((t) * 16 + (((s) + (t)) & 15))

extern __shared__ __attribute__((aligned(16))) unsigned char smem[];

__device__ __forceinline__ float lo2f(u32 p) { return __uint_as_float(p << 16); }
__device__ __forceinline__ float hi2f(u32 p) { return __uint_as_float(p & 0xFFFF0000u); }
__device__ __forceinline__ u32 pk(float a, float b) {
  bf16x2 r = {(__bf16)a, (__bf16)b};
  return __builtin_bit_cast(u32, r);
}
__device__ __forceinline__ float rbf(float f) { return lo2f(pk(f, f)); }
__device__ __forceinline__ float sel4(u32 g, float a, float b, float c, float d) {
  const bool b0 = g & 1, b1 = g & 2;
  const float lo = b0 ? b : a, hi = b0 ? d : c;
  return b1 ? hi : lo;
}
__device__ __forceinline__ rsrc_t mk(const void* p, u32 bytes) {
  return __builtin_amdgcn_make_buffer_rsrc((void*)p, 0, bytes, 0x00020000);
}
__device__ __forceinline__ float dot2(u32 w, u32 x, float acc) {
  return __builtin_amdgcn_fdot2_f32_bf16(__builtin_bit_cast(bf16x2, w), __builtin_bit_cast(bf16x2, x), acc, false);
}
template <int CTRL> __device__ __forceinline__ float dpp(float x) {
  return __builtin_bit_cast(float, __builtin_amdgcn_mov_dpp(__builtin_bit_cast(int, x), CTRL, 0xf, 0xf, true));
}
constexpr int DPP_XOR1 = 0xB1, DPP_XOR2 = 0x4E, DPP_HMIRROR = 0x141, DPP_MIRROR = 0x140, DPP_ROR4 = 0x124, DPP_ROR8 = 0x128;
__device__ __forceinline__ float swap16_add(float a, float b) {
  auto s = __builtin_amdgcn_permlane16_swap(__float_as_uint(a), __float_as_uint(b), false, false);
  return __uint_as_float(s[0]) + __uint_as_float(s[1]);
}
__device__ __forceinline__ float swap32_add(float a, float b) {
  auto s = __builtin_amdgcn_permlane32_swap(__float_as_uint(a), __float_as_uint(b), false, false);
  return __uint_as_float(s[0]) + __uint_as_float(s[1]);
}
__device__ __forceinline__ float wave_sum(float x) {
  x += dpp<DPP_XOR1>(x); x += dpp<DPP_XOR2>(x); x += dpp<DPP_HMIRROR>(x); x += dpp<DPP_MIRROR>(x);
  x = swap16_add(x, x); x = swap32_add(x, x);
  return x;
}
__device__ __forceinline__ float wave_max(float x) {
  x = fmaxf(x, dpp<DPP_XOR1>(x)); x = fmaxf(x, dpp<DPP_XOR2>(x)); x = fmaxf(x, dpp<DPP_HMIRROR>(x)); x = fmaxf(x, dpp<DPP_MIRROR>(x));
  auto s = __builtin_amdgcn_permlane16_swap(__float_as_uint(x), __float_as_uint(x), false, false);
  x = fmaxf(__uint_as_float(s[0]), __uint_as_float(s[1]));
  auto t = __builtin_amdgcn_permlane32_swap(__float_as_uint(x), __float_as_uint(x), false, false);
  return fmaxf(__uint_as_float(t[0]), __uint_as_float(t[1]));
}
__device__ __forceinline__ float silu_b(float v) {
  return rbf(v * __builtin_amdgcn_rcpf(1.f + __builtin_amdgcn_exp2f(-1.4426950408889634f * v)));
}

struct Tag { u32 t0, t1, t2, t3, vm; };
__device__ __forceinline__ u32 q22r(u32 rounded, u32 vm, u32 t) { return __builtin_amdgcn_bitop3_b32(rounded, vm, t, 0xEA); }
__device__ __forceinline__ u32 rnd22(float v) { return __float_as_uint(v) + 0x200u; }
__device__ __forceinline__ float r22(float v) { return __uint_as_float(rnd22(v) & 0xFFFFFC00u); }
__device__ __forceinline__ v4u quad(float a, float b, float c, float d, Tag t) {
  return v4u{q22r(rnd22(a), t.vm, t.t0), q22r(rnd22(b), t.vm, t.t1), q22r(rnd22(c), t.vm, t.t2), q22r(rnd22(d), t.vm, t.t3)};
}
__device__ __forceinline__ v4u quad_of_lanes(float v, Tag tag) {
  const float x = __uint_as_float(rnd22(v));
  return v4u{q22r(__float_as_uint(dpp<0x00>(x)), tag.vm, tag.t0), q22r(__float_as_uint(dpp<0x55>(x)), tag.vm, tag.t1),
             q22r(__float_as_uint(dpp<0xAA>(x)), tag.vm, tag.t2), q22r(__float_as_uint(dpp<0xFF>(x)), tag.vm, tag.t3)};
}
__device__ __forceinline__ float gv(u32 d) { return __uint_as_float(d & 0xFFFFFC00u); }
__device__ __forceinline__ f32x4 gv4(v4u v) { return f32x4{gv(v.x), gv(v.y), gv(v.z), gv(v.w)}; }
__device__ __forceinline__ void st_quad(rsrc_t rs, u32 off, v4u q) { __builtin_amdgcn_raw_buffer_store_b128(q, rs, off, 0, AUX_SC1); }
template <int N, class Off, class Use>
__device__ __forceinline__ void gather(rsrc_t rws, Tag tag, Off off, Use use) {
  v4u a[N];
#pragma unroll 1
  for (u32 sp = 0; sp < SPIN_CAP; ++sp) {
    asm volatile("" ::: "memory");
#pragma unroll
    for (int i = 0; i < N; ++i) a[i] = __builtin_amdgcn_raw_buffer_load_b128(rws, off(i), 0, AUX_SC1);
    u32 bad = 0;
#pragma unroll
    for (int i = 0; i < N; ++i) {
      bad = __builtin_amdgcn_bitop3_b32(a[i].x, tag.t0, bad, 0xBE); bad = __builtin_amdgcn_bitop3_b32(a[i].y, tag.t1, bad, 0xBE);
      bad = __builtin_amdgcn_bitop3_b32(a[i].z, tag.t2, bad, 0xBE); bad = __builtin_amdgcn_bitop3_b32(a[i].w, tag.t3, bad, 0xBE);
    }
    if (__all((bad & 0x3FFu) == 0u)) break;
  }
  use(a);
}

__device__ __forceinline__ volatile LAS u32* flg(int i) { return (volatile LAS u32*)(smem + L_FLG) + i; }
__device__ __forceinline__ void wait_flag(int i, u32 want) {
  volatile LAS u32* p = flg(i);
#pragma unroll 1
  for (u32 s = 0; s < SPIN_CAP; ++s) {
    if (__builtin_amdgcn_readfirstlane(*p) >= want) break;
    __builtin_amdgcn_s_sleep(1);
  }
  asm volatile("" ::: "memory");
}
__device__ __forceinline__ void wait_flag_tight(int i, u32 want) {
  volatile LAS u32* p = flg(i);
#pragma unroll 1
  for (u32 s = 0; s < SPIN_CAP; ++s) {
    if (__builtin_amdgcn_readfirstlane(*p) >= want) break;
  }
  asm volatile("" ::: "memory");
}
__device__ __forceinline__ void set_flag(int i) {
  asm volatile("s_waitcnt lgkmcnt(0)" ::: "memory");
  *flg(i) = 1u;
  asm volatile("" ::: "memory");
}
__device__ __forceinline__ void add_flag(int i) {
  asm volatile("s_waitcnt lgkmcnt(0)" ::: "memory");
  if ((threadIdx.x & 63) == 0) __hip_atomic_fetch_add((LAS u32*)(smem + L_FLG) + i, 1u, __ATOMIC_RELAXED, __HIP_MEMORY_SCOPE_WORKGROUP);
  asm volatile("" ::: "memory");
}
__device__ __forceinline__ u32 bump_flag(int i) {
  asm volatile("s_waitcnt lgkmcnt(0)" ::: "memory");
  u32 r = 0;
  if ((threadIdx.x & 63) == 0) r = __hip_atomic_fetch_add((LAS u32*)(smem + L_FLG) + i, 1u, __ATOMIC_RELAXED, __HIP_MEMORY_SCOPE_WORKGROUP);
  r = __builtin_amdgcn_readfirstlane(r);
  asm volatile("" ::: "memory");
  return r;
}
__device__ __forceinline__ Tag gettag() {
  wait_flag(FL_TAG, 1);
  const v4u tg = *(const LAS v4u*)(smem + L_TAG);
  Tag t{(u32)__builtin_amdgcn_readfirstlane(tg.x), (u32)__builtin_amdgcn_readfirstlane(tg.y), (u32)__builtin_amdgcn_readfirstlane(tg.z),
        (u32)__builtin_amdgcn_readfirstlane(tg.w), 0xFFFFFC00u};
  asm volatile("" : "+s"(t.t0), "+s"(t.t1), "+s"(t.t2), "+s"(t.t3), "+v"(t.vm));
  return t;
}
__device__ __forceinline__ void dma16(rsrc_t rs, u32 voff, u32 soff, u32 ldsoff) {
  asm volatile("s_mov_b32 m0, %0\n\ts_nop 0\n\tbuffer_load_dwordx4 %1, %2, %3 offen nt lds" ::"s"(ldsoff), "v"(voff), "s"(rs), "s"(soff) : "m0", "memory");
}
#define VMCNT_(n) asm volatile("s_waitcnt vmcnt(" #n ")" ::: "memory")
#define VMCNT(n) VMCNT_(n)
#define SB() __builtin_amdgcn_sched_barrier(0)

struct Args {
  const bf16_t* x; const bf16_t* kc; const bf16_t* vc; const bf16_t* wq; const bf16_t* wo;
  const bf16_t* wg; const bf16_t* wu; const bf16_t* wd; bf16_t* out; unsigned char* ws;
};

__device__ __forceinline__ void down_gemv(int part, int j, int lane, Tag tag, rsrc_t rws) {
  const int r = j >> 2, c4 = j & 3;
  const u32 lr = lane >> 4, q4 = (lane & 15) >> 2, p4 = lane & 3;
  const u32 badr = L_K + (8 * lr + q4) * 288 + p4 * 8 + part * 96;
  const u32 row4 = 64 + 4 * lr + q4;
  const u32 badr4 = L_K + (row4 < 72 ? row4 : 71) * 288 + p4 * 8 + part * 96;
  const u32 cq = c4 * 36 + (3 * part + lr) * 4 + q4;
  u32 off = (lr < 3 && p4 == 0) ? WS_FIN + (((cq >> 1) * 2 + 1) * 64 + r) * 32 + (cq & 1) * 16 : OOB;
  u32 aadr = L_HI + lr * 16, aadr2 = L_HI + 128 + lr * 8;
  asm volatile("" : "+v"(off), "+v"(aadr), "+v"(aadr2));
  wait_flag(FL_DL, 1);
  s16x4 b[3][5];
#pragma unroll
  for (int t = 0; t < 3; ++t) {
#pragma unroll
    for (int u = 0; u < 4; ++u) b[t][u] = __builtin_amdgcn_ds_read_tr16_b64_v4i16((LAS s16x4*)(smem + badr + ((u >> 1) * 32 + (u & 1) * 4) * 288 + t * 32));
    b[t][4] = __builtin_amdgcn_ds_read_tr16_b64_v4i16((LAS s16x4*)(smem + badr4 + t * 32));
  }
#pragma unroll
  for (int t = 0; t < 3; ++t)
#pragma unroll
    for (int u = 0; u < 5; ++u) asm volatile("" : "+v"(b[t][u]));
  wait_flag_tight(FL_H, 2);
  const s16x8 a0 = *(const LAS s16x8*)(smem + aadr), a1 = *(const LAS s16x8*)(smem + aadr + 64);
  const s16x4 a2 = *(const LAS s16x4*)(smem + aadr2);
  SB();
  f32x4 acc[3];
#pragma unroll
  for (int t = 0; t < 3; ++t)
    acc[t] = __builtin_amdgcn_mfma_f32_16x16x32_bf16(__builtin_bit_cast(bf16x8, a0), __builtin_bit_cast(bf16x8, __builtin_shufflevector(b[t][0], b[t][1], 0, 1, 2, 3, 4, 5, 6, 7)), f32x4{0.f, 0.f, 0.f, 0.f}, 0, 0, 0);
  SB();
#pragma unroll
  for (int t = 0; t < 3; ++t)
    acc[t] = __builtin_amdgcn_mfma_f32_16x16x32_bf16(__builtin_bit_cast(bf16x8, a1), __builtin_bit_cast(bf16x8, __builtin_shufflevector(b[t][2], b[t][3], 0, 1, 2, 3, 4, 5, 6, 7)), acc[t], 0, 0, 0);
  SB();
#pragma unroll
  for (int t = 0; t < 3; ++t) acc[t] = __builtin_amdgcn_mfma_f32_16x16x16bf16_1k(a2, b[t][4], acc[t], 0, 0, 0);
  SB();
  st_quad(rws, off, quad_of_lanes(sel4(lr, acc[0][0], acc[1][0], acc[2][0], acc[2][0]), tag));
  add_flag(FL_FIN);
}

__global__ void __launch_bounds__(NTHR, 3) fused_kernel(Args a) {
  const int tid = threadIdx.x, lane = tid & 63;
  const int wave = __builtin_amdgcn_readfirstlane(tid >> 6);
  const int b = blockIdx.x;
  const int j = (b & 7) * 32 + (b >> 3);
  if (wave == 11) {
    if (lane < 32) *flg(lane) = 0u;
    if (lane >= 32 && lane < 36) *(LAS u32*)(smem + L_HI + 144 + (lane - 32) * 4) = 0u;
  }
  SB();
  asm volatile("s_waitcnt lgkmcnt(0)" ::: "memory");
  __builtin_amdgcn_s_barrier();
  asm volatile("" ::: "memory");
  SB();

  if (__builtin_expect(wave < 4, 1)) {
    __builtin_amdgcn_s_setprio(3);
    const int kp = j & 3, r = j >> 2;
    const bf16_t* wm; u32 row0, stride, strip, nrows, pub;
    if (wave == 0) { wm = a.wq; row0 = 144 * kp; stride = NQ * 2; strip = r; nrows = 144; pub = WS_Q + j * 256; }
    else if (wave < 3) {
      const int quarter = 2 * (kp & 1) + (wave - 1);
      wm = (kp >> 1) ? a.wu : a.wg; row0 = 144 * quarter; stride = F * 2; nrows = 144;
      strip = (r & ~7) | ((r + 3 * wave) & 7);
      pub = WS_GM + ((strip * 2 + (kp >> 1)) * 4 + quarter) * 256;
    } else { wm = ((j >> 4) & 1) ? a.wu : a.wg; row0 = 36 * (j & 15); stride = F * 2; strip = 64 + (j >> 5); nrows = 36; pub = WS_GP + j * 256; }
    const rsrc_t rw = mk((const unsigned char*)wm + (size_t)row0 * stride + strip * 128, (nrows - 1) * stride + 128);
    const rsrc_t rx = mk(a.x, D * 2);
    const rsrc_t rws = mk(a.ws, WS_END);
    u64 tg = __hip_atomic_load((GAS u64*)(a.ws + WS_EPOCH), __ATOMIC_RELAXED, __HIP_MEMORY_SCOPE_AGENT);
    u32 xr[5];
#pragma unroll
    for (int i = 0; i < 5; ++i) xr[i] = __builtin_amdgcn_raw_buffer_load_b32(rx, lane * 4, i * 256, 0);
    const u32 rs8 = lane >> 3;
    const u32 voff = rs8 * stride + (lane & 7) * 16;
    Tag tag;
    auto layernorm = [&](v4u (&av)[18]) {
#pragma unroll
      for (int i = 0; i < 5; ++i) asm volatile("" : "+v"(xr[i]));
      asm volatile("" : "+v"(tg));
      {
        tg += 1;
        const u64 T = ((u64)(u32)__builtin_amdgcn_readfirstlane((u32)(tg >> 32)) << 32) | (u32)__builtin_amdgcn_readfirstlane((u32)tg);
        const u64 H = T * 0x9E3779B97F4A7C15ull;
        tag = Tag{(u32)T & 0x3FFu, (u32)(H >> 54), (u32)(H >> 44) & 0x3FFu, (u32)(H >> 34) & 0x3FFu, 0xFFFFFC00u};
        asm volatile("" : "+v"(tag.vm));
        if (wave == 0) {
          if (lane == 0) { *(LAS v4u*)(smem + L_TAG) = v4u{tag.t0, tag.t1, tag.t2, tag.t3}; *(LAS v2u*)(smem + L_TAG + 16) = v2u{(u32)T, (u32)(T >> 32)}; }
          set_flag(FL_TAG);
        }
      }
      float s = 0.f;
#pragma unroll
      for (int i = 0; i < 5; ++i) s = dot2(xr[i], 0x3F803F80u, s);
      const float mean = wave_sum(s) * (1.f / 576.f);
      float q = 0.f, q4 = 0.f;
#pragma unroll
      for (int i = 0; i < 4; ++i) { const float d0 = lo2f(xr[i]) - mean, d1 = hi2f(xr[i]) - mean; q = fmaf(d0, d0, q); q = fmaf(d1, d1, q); }
      { const float d0 = lo2f(xr[4]) - mean, d1 = hi2f(xr[4]) - mean; q4 = fmaf(d0, d0, d1 * d1); }
      q += lane < 32 ? q4 : 0.f;
      const float var = rbf(wave_sum(q) * (1.f / 576.f));
      const float mb = rbf(mean);
      const float inv = __builtin_amdgcn_rcpf(rbf(__builtin_amdgcn_sqrtf(rbf(var + 1e-5f))));
      LAS v2u* xn = (LAS v2u*)(smem + L_XN);
#pragma unroll
      for (int i = 0; i < 5; ++i) {
        const u32 d = pk(lo2f(xr[i]) - mb, hi2f(xr[i]) - mb);
        const u32 n = pk(lo2f(d) * inv, hi2f(d) * inv);
        if (i < 4 || lane < 32) xn[i * 64 + lane] = v2u{__builtin_amdgcn_perm(n, n, 0x01000100u), __builtin_amdgcn_perm(n, n, 0x03020302u)};
      }
      asm volatile("s_waitcnt lgkmcnt(0)" ::: "memory");
      u32 xp[18];
      const LAS u32* xs = (const LAS u32*)(smem + L_XN) + row0 + rs8;
#pragma unroll
      for (int i = 0; i < 18; ++i) xp[i] = xs[(wave == 3 && i > 4) ? 0 : 8 * i];
      if (wave == 3 && rs8 >= 4) xp[4] = 0u;
      u32 msk[4];
#pragma unroll
      for (int d = 0; d < 4; ++d) msk[d] = (((lane & 7) >> 1) == d) ? ((lane & 1) ? 0xFFFF0000u : 0x0000FFFFu) : 0u;
#pragma unroll
      for (int i = 0; i < 18; ++i) av[i] = v4u{xp[i] & msk[0], xp[i] & msk[1], xp[i] & msk[2], xp[i] & msk[3]};
    };
    auto gemv_publish = [&](const v4u (&av)[18], const v4u (&w)[18]) {
      f32x4 da = {0.f, 0.f, 0.f, 0.f}, db = {0.f, 0.f, 0.f, 0.f};
#pragma unroll
      for (int i = 0; i < 18; ++i) {
        if (i & 1) db = __builtin_amdgcn_mfma_f32_16x16x32_bf16(__builtin_bit_cast(bf16x8, av[i]), __builtin_bit_cast(bf16x8, w[i]), db, 0, 0, 0);
        else da = __builtin_amdgcn_mfma_f32_16x16x32_bf16(__builtin_bit_cast(bf16x8, av[i]), __builtin_bit_cast(bf16x8, w[i]), da, 0, 0, 0);
      }
      da += db;
      f32x4 res;
#pragma unroll
      for (int k = 0; k < 4; ++k) {
        const float up = dpp<DPP_ROR8>(da[k]);
        res[k] = da[k] + __uint_as_float(__builtin_amdgcn_permlane32_swap(__float_as_uint(up), __float_as_uint(up), false, false)[1]);
      }
      const u32 off = ((lane & 0x28) == 0) ? pub + (2 * (lane & 7) + (lane >> 4)) * 16 : OOB;
      st_quad(rws, off, quad(res[0], res[1], res[2], res[3], tag));
    };
    if (__builtin_expect(wave == 0, 1)) {
      v4u w[18], av[18];
#pragma unroll
      for (int i = 0; i < 18; ++i) w[i] = __builtin_amdgcn_raw_buffer_load_b128(rw, voff, 8 * i * stride, AUX_NT);
      SB();
      set_flag(FL_W0);
      layernorm(av);
      gemv_publish(av, w);
      set_flag(FL_QPUB);
      {
        const u32 bm = WS_GM + r * 2048 + lane * 16;
        wait_flag(FL_HPUB, 3);
        __builtin_amdgcn_s_sleep(H_DELAY);
        wait_flag(FL_DH, 1);
        gather<2>(rws, tag, [&](int i) { return bm + i * 1024; }, [&](const v4u (&g)[2]) {
          const f32x4 gg = gv4(g[0]), uu = gv4(g[1]);
          const float gs = swap16_add(swap32_add(gg[0], gg[2]), swap32_add(gg[1], gg[3]));
          const float us = swap16_add(swap32_add(uu[0], uu[2]), swap32_add(uu[1], uu[3]));
          const float hv = silu_b(rbf(gs)) * rbf(us);
          ((LAS bf16_t*)(smem + L_HI))[(lane & 15) * 4 + (lane >> 4)] = (bf16_t)(pk(hv, hv) & 0xFFFFu);
          add_flag(FL_H);
        });
      }
      if ((j & 31) < 9) {
        const int f = (j >> 5) * 9 + (j & 31);
        const u32 mycol = f * 8 + 4 * (lane & 1) + 2 * ((lane >> 4) & 1) + ((lane >> 3) & 1);
        const u32 xo = __builtin_amdgcn_raw_buffer_load_b16(rx, mycol * 2, 0, 0);
        const u32 base = WS_FIN + f * 4096 + lane * 16;
        wait_flag(FL_FIN, 5);
        __builtin_amdgcn_s_sleep(F_DELAY);
        gather<4>(rws, tag, [&](int i) { return base + i * 1024; }, [&](const v4u (&g)[4]) {
          const f32x4 sv = gv4(g[0]) + gv4(g[1]), fv = gv4(g[2]) + gv4(g[3]);
          float a4[4];
#pragma unroll
          for (int k = 0; k < 4; ++k) a4[k] = swap32_add(sv[k], fv[k]);
          const float a0 = swap16_add(a4[0], a4[2]), a1 = swap16_add(a4[1], a4[3]);
          float b0 = a0 + dpp<DPP_XOR2>(a0), b1 = a1 + dpp<DPP_XOR2>(a1);
          b0 += dpp<DPP_ROR4>(b0); b1 += dpp<DPP_ROR4>(b1);
          b0 += dpp<DPP_ROR8>(b0); b1 += dpp<DPP_ROR8>(b1);
          const float t = (lane & 8) ? b1 : b0;
          const auto sf = __builtin_amdgcn_permlane32_swap(__float_as_uint(t), __float_as_uint(t), false, false);
          const float sb = rbf(__uint_as_float(sf[0])), fb = rbf(__uint_as_float(sf[1]));
          const float o = lo2f(xo) + rbf(sb * silu_b(fb));
          if ((lane & 0x26) == 0) *(GAS bf16_t*)((GAS unsigned char*)a.out + mycol * 2) = (bf16_t)(pk(o, o) & 0xFFFFu);
        });
        if (f == 0 && lane == 0) {
          const v2u nx = *(const LAS v2u*)(smem + L_TAG + 16);
          __hip_atomic_store((GAS u64*)(a.ws + WS_EPOCH), (u64)nx.x | ((u64)nx.y << 32), __ATOMIC_RELAXED, __HIP_MEMORY_SCOPE_AGENT);
        }
      }
    } else {
      v4u w[18], av[18];
      layernorm(av);
      __builtin_amdgcn_s_setprio(1);
#pragma unroll
      for (int i = 0; i < 18; ++i) asm volatile("" : "+v"(av[i]));
      wait_flag(FL_VISS, 4);
#pragma unroll
      for (int i = 0; i < 18; ++i) w[i] = __builtin_amdgcn_raw_buffer_load_b128(rw, voff, 8 * i * stride, AUX_NT);
      SB();
      add_flag(FL_G0);
      __builtin_amdgcn_s_setprio(3);
      gemv_publish(av, w);
      add_flag(FL_HPUB);
      if (wave == 1) {
        const u32 bp = WS_GP + ((r >> 3) * 32 + (lane >> 1)) * 256 + ((r & 7) * 2 + (lane & 1)) * 16;
        wait_flag(FL_HPUB, 3);
        __builtin_amdgcn_s_sleep(H_DELAY);
        wait_flag(FL_DH, 1);
        gather<1>(rws, tag, [&](int) { return bp; }, [&](const v4u (&g)[1]) {
          f32x4 t = gv4(g[0]);
#pragma unroll
          for (int k = 0; k < 4; ++k) t[k] += dpp<DPP_XOR2>(t[k]);
          float a0 = swap16_add(t[0], t[2]), a1 = swap16_add(t[1], t[3]);
          a0 += dpp<DPP_ROR4>(a0); a1 += dpp<DPP_ROR4>(a1);
          a0 += dpp<DPP_ROR8>(a0); a1 += dpp<DPP_ROR8>(a1);
          const float v = (lane & 8) ? a1 : a0;
          const auto gu = __builtin_amdgcn_permlane32_swap(__float_as_uint(v), __float_as_uint(v), false, false);
          const float hv = silu_b(rbf(__uint_as_float(gu[0]))) * rbf(__uint_as_float(gu[1]));
          if ((lane & 0x26) == 0) ((LAS bf16_t*)(smem + L_HI))[64 + 4 * (lane & 1) + 2 * ((lane >> 4) & 1) + ((lane >> 3) & 1)] = (bf16_t)(pk(hv, hv) & 0xFFFFu);
          add_flag(FL_H);
        });
      } else {
        down_gemv(wave - 1, j, lane, tag, rws);
      }
    }
  } else if (__builtin_expect(wave < 8, 1)) {
    __builtin_amdgcn_s_setprio(0);
    const int w = wave - 4, g = j >> 4, s = j & 15;
    const rsrc_t rk = mk(a.kc + ((size_t)g * SK + (SK - WIN)) * DH, WIN * DH * 2);
    const rsrc_t rv = mk(a.vc + ((size_t)g * SK + (SK - WIN)) * DH, WIN * DH * 2);
    const rsrc_t rws = mk(a.ws, WS_END);
    const u32 lr = lane >> 4;
    u32 vo[4];
#pragma unroll
    for (int sub = 0; sub < 4; ++sub) { vo[sub] = sub * 1024 + lr * 256 + ((((lane & 15) ^ (lr << 2)) ^ sub) << 4); asm volatile("" : "+v"(vo[sub])); }
    wait_flag(FL_W0, 1);
#pragma unroll
    for (int p = 0; p < 16; ++p) dma16(rk, vo[p & 3], KVTILE(4 * w + (p >> 2), s) * 4096, L_K + w * 16384 + p * 1024);
#pragma unroll
    for (int p = 0; p < 16; ++p) {
      dma16(rv, vo[p & 3], KVTILE(4 * w + (p >> 2), s) * 4096, L_V + w * 16384 + p * 1024);
      if (p == VH_AT && w == 0) *flg(FL_VH) = 1u;
    }
    add_flag(FL_VISS);
    __builtin_amdgcn_s_setprio(3);
    const Tag tag = gettag();
    const u32 krow = lane & 15;
    u32 kadr = L_K + w * 16384 + krow * 256 + ((lr ^ (((krow & 3) << 2) | ((krow >> 2) & 3))) << 4);
    const u32 q4 = (lane & 15) >> 2, p4 = lane & 3;
    u32 va[2][8];
#pragma unroll
    for (int t = 0; t < 2; ++t) {
      const u32 row = 8 * (lr & 1) + 4 * t + q4;
      const u32 base = L_V + w * 16384 + (lr >> 1) * 4096 + row * 256 + (((p4 >> 1) ^ ((q4 << 2) | (2 * (lr & 1) + t))) << 4) + 8 * (p4 & 1);
#pragma unroll
      for (int c = 0; c < 8; ++c) { va[t][c] = base ^ (c << 5); asm volatile("" : "+v"(va[t][c])); }
    }
    u32 ob = WS_OP + (((2 * g + (lane >> 5)) * 16 + s) * 32 + (lane & 31)) * 16;
    u32 mlb = (lane & 31) == 0 ? WS_ML + ((2 * g + (lane >> 5)) * 16 + s) * 16 : OOB;
    u32 oadr = L_OW + lane * 16, ladr = L_MX + 32 + (lane >> 5) * 4;
    asm volatile("" : "+v"(kadr), "+v"(ob), "+v"(mlb), "+v"(oadr), "+v"(ladr));
    VMCNT(16);
    s16x8 kf[16];
#pragma unroll
    for (int i = 0; i < 4; ++i)
#pragma unroll
      for (int st = 0; st < 4; ++st) kf[i * 4 + st] = *(const LAS s16x8*)(smem + ((kadr ^ (st << 6)) + i * 4096));
#pragma unroll
    for (int i = 0; i < 16; ++i) asm volatile("" : "+v"(kf[i]));
    add_flag(FL_MX);
    wait_flag(FL_Q, 1);
    s16x8 qf[4];
#pragma unroll
    for (int st = 0; st < 4; ++st) qf[st] = *(const LAS s16x8*)(smem + L_QI + (lane & 1) * 256 + st * 64 + lr * 16);
    f32x4 sc[4];
#pragma unroll
    for (int i = 0; i < 4; ++i) sc[i] = f32x4{0.f, 0.f, 0.f, 0.f};
#pragma unroll
    for (int st = 0; st < 4; ++st)
#pragma unroll
      for (int i = 0; i < 4; ++i)
        sc[i] = __builtin_amdgcn_mfma_f32_16x16x32_bf16(__builtin_bit_cast(bf16x8, qf[st]), __builtin_bit_cast(bf16x8, kf[i * 4 + st]), sc[i], 0, 0, 0);
    constexpr float C2 = 0.08838834764831845f * 1.4426950408889634f;
    const float s0 = sel4(lr, sc[0][0], sc[1][0], sc[2][0], sc[3][0]) * C2;
    const float s1 = sel4(lr, sc[0][1], sc[1][1], sc[2][1], sc[3][1]) * C2;
    LAS float* mx = (LAS float*)(smem + L_MX);
    {
      const float wm0 = wave_max(s0), wm1 = wave_max(s1);
      if (lane == 0) *(LAS v2u*)(mx + 2 * w) = v2u{__float_as_uint(wm0), __float_as_uint(wm1)};
    }
    add_flag(FL_M2);
    wait_flag_tight(FL_M2, 4);
    const f32x4 ma = *(const LAS f32x4*)(mx), mb4 = *(const LAS f32x4*)(mx + 4);
    const float m0 = r22(fmaxf(fmaxf(ma[0], ma[2]), fmaxf(mb4[0], mb4[2])));
    const float m1 = r22(fmaxf(fmaxf(ma[1], ma[3]), fmaxf(mb4[1], mb4[3])));
    const u32 pp = pk(__builtin_amdgcn_exp2f(s0 - m0), __builtin_amdgcn_exp2f(s1 - m1));
    LAS bf16_t* pi = (LAS bf16_t*)(smem + L_PI + w * 256);
    pi[lane] = (bf16_t)(pp & 0xFFFFu); pi[64 + lane] = (bf16_t)(pp >> 16);
    {
      const float l0 = wave_sum(lo2f(pp)), l1 = wave_sum(hi2f(pp));
      if (lane == 0) *(LAS v2u*)(mx + 8 + 2 * w) = v2u{__float_as_uint(l0), __float_as_uint(l1)};
    }
    s16x8 pf[2];
#pragma unroll
    for (int ks = 0; ks < 2; ++ks) pf[ks] = *(const LAS s16x8*)(smem + L_PI + w * 256 + (lane & 1) * 128 + ks * 64 + lr * 16);
    f32x4 oc[8];
#pragma unroll
    for (int c = 0; c < 8; ++c) oc[c] = f32x4{0.f, 0.f, 0.f, 0.f};
#pragma unroll
    for (int ks = 0; ks < 2; ++ks) {
      if (ks == 0) VMCNT(8); else VMCNT(0);
      s16x4 b0[8], b1[8];
#pragma unroll
      for (int c = 0; c < 8; ++c) {
        b0[c] = __builtin_amdgcn_ds_read_tr16_b64_v4i16((LAS s16x4*)(smem + va[0][c] + ks * 8192));
        b1[c] = __builtin_amdgcn_ds_read_tr16_b64_v4i16((LAS s16x4*)(smem + va[1][c] + ks * 8192));
      }
      SB();
#pragma unroll
      for (int c = 0; c < 8; ++c) {
        const s16x8 bb = __builtin_shufflevector(b0[c], b1[c], 0, 1, 2, 3, 4, 5, 6, 7);
        oc[c] = __builtin_amdgcn_mfma_f32_16x16x32_bf16(__builtin_bit_cast(bf16x8, pf[ks]), __builtin_bit_cast(bf16x8, bb), oc[c], 0, 0, 0);
      }
      SB();
    }
    if (lane < 16) {
      LAS float* ow = (LAS float*)(smem + L_OW) + w * 256 + lane;
#pragma unroll
      for (int c = 0; c < 8; ++c) { ow[c * 16] = oc[c][0]; ow[128 + c * 16] = oc[c][1]; }
    }
    if (bump_flag(FL_OW) == 3) {
      f32x4 t[4];
#pragma unroll
      for (int u = 0; u < 4; ++u) t[u] = *(const LAS f32x4*)(smem + oadr + u * 1024);
      const float l0 = *(const LAS float*)(smem + ladr), l1 = *(const LAS float*)(smem + ladr + 8), l2 = *(const LAS float*)(smem + ladr + 16), l3 = *(const LAS float*)(smem + ladr + 24);
      SB();
      const f32x4 o = (t[0] + t[1]) + (t[2] + t[3]);
      st_quad(rws, ob, quad(o[0], o[1], o[2], o[3], tag));
      st_quad(rws, mlb, quad((lane >> 5) ? m1 : m0, (l0 + l1) + (l2 + l3), 0.f, 0.f, tag));
      set_flag(FL_OPUB);
    }
  } else if (wave < 10) {
    __builtin_amdgcn_s_setprio(1);
    const int w2 = wave & 1;
    const int r = j >> 2, c4 = j & 3;
    const rsrc_t rw = mk(a.wo, NQ * D * 2);
    const rsrc_t rws = mk(a.ws, WS_END);
    v4u warm[4];
    if (w2 == 0) {
      const int k = j & 31, x = j >> 5;
      const unsigned char* p = (const unsigned char*)a.x;
      if (k < 2) p = (const unsigned char*)(a.vc + ((size_t)(2 * x + k) * SK + (SK - WIN)) * DH);
      else if (k < 4) p = (const unsigned char*)(a.kc + ((size_t)(2 * x + k - 2) * SK + (SK - WIN)) * DH);
      else if (k < 7) p = (const unsigned char*)a.wg + (size_t)(k - 4) * (2u << 20) + 1024 * x;
      else if (k < 10) p = (const unsigned char*)a.wu + (size_t)(k - 7) * (2u << 20) + 1024 * x;
      else if (k < 12) p = (const unsigned char*)a.wo + (size_t)(512 * x + 511 * (k - 10)) * 1152;
      else if (k < 14) p = (const unsigned char*)a.wd + (size_t)(512 * x + 511 * (k - 12)) * 1152;
      else if (k == 14) p = (const unsigned char*)a.wd + (size_t)(4096 + 64 * x) * 1152;
      else if (k < 18) p = (const unsigned char*)a.wq + (size_t)(k - 15) * (2u << 20) + 1024 * x;
      const u32 v = k < 18 ? *(const GAS u32*)p : 0u;
      warm[0] = warm[1] = warm[2] = warm[3] = v4u{v, v, v, v};
    } else {
      constexpr u32 SL = 3328;
      static_assert(256 * SL >= WS_END - WS_Q, "hand-off region");
#pragma unroll
      for (int i = 0; i < 4; ++i) warm[i] = __builtin_amdgcn_raw_buffer_load_b128(rws, (u32)(i * 1024 + lane * 16) < SL ? WS_Q + j * SL + i * 1024 + lane * 16 : OOB, 0, 0);
    }
    u32 vo[9];
#pragma unroll
    for (int i = 0; i < 9; ++i) {
      const u32 id = (w2 + 2 * i) * 64 + lane;
      const u32 row = (id * 3641u) >> 16, c = id - row * 18;
      vo[i] = (64 * r + row) * 1152 + c4 * 288 + c * 16;
      asm volatile("" : "+v"(vo[i]));
    }
    const Tag tag = gettag();
#pragma unroll
    for (int i = 0; i < 4; ++i) asm volatile("" ::"v"(warm[i]));
    wait_flag(FL_G0, 3);
#pragma unroll
    for (int i = 0; i < 9; ++i) dma16(rw, vo[i], 0, L_WO + (w2 + 2 * i) * 1024);
    __builtin_amdgcn_s_setprio(3);
    VMCNT(0);
    add_flag(FL_WOL);
    wait_flag(FL_WOL, 2);
    const u32 lr = lane >> 4, q4 = (lane & 15) >> 2, p4 = lane & 3;
    const u32 badr = L_WO + (8 * lr + q4) * 288 + p4 * 8 + w2 * 32;
    s16x4 bb[5][4];
#pragma unroll
    for (int t = 0; t < 5; ++t)
#pragma unroll
      for (int u = 0; u < 4; ++u)
        bb[t][u] = __builtin_amdgcn_ds_read_tr16_b64_v4i16((LAS s16x4*)(smem + badr + ((u >> 1) * 32 + (u & 1) * 4) * 288 + ((t == 4 && w2) ? 3 : t) * 64));
    const u32 cqA = c4 * 36 + (w2 + 2 * lr) * 4 + q4, cqB = c4 * 36 + 32 + q4;
    u32 offA = p4 == 0 ? WS_FIN + (((cqA >> 1) * 2) * 64 + r) * 32 + (cqA & 1) * 16 : OOB;
    u32 offB = (w2 == 0 && lr == 0 && p4 == 0) ? WS_FIN + (((cqB >> 1) * 2) * 64 + r) * 32 + (cqB & 1) * 16 : OOB;
    u32 aadr = L_OI + lr * 16;
    asm volatile("" : "+v"(offA), "+v"(offB), "+v"(aadr));
#pragma unroll
    for (int t = 0; t < 5; ++t)
#pragma unroll
      for (int u = 0; u < 4; ++u) asm volatile("" : "+v"(bb[t][u]));
    wait_flag_tight(FL_O, 1);
    const s16x8 a0 = *(const LAS s16x8*)(smem + aadr), a1 = *(const LAS s16x8*)(smem + aadr + 64);
    SB();
    f32x4 acc[5];
#pragma unroll
    for (int t = 0; t < 5; ++t)
      acc[t] = __builtin_amdgcn_mfma_f32_16x16x32_bf16(__builtin_bit_cast(bf16x8, a0), __builtin_bit_cast(bf16x8, __builtin_shufflevector(bb[t][0], bb[t][1], 0, 1, 2, 3, 4, 5, 6, 7)), f32x4{0.f, 0.f, 0.f, 0.f}, 0, 0, 0);
    SB();
#pragma unroll
    for (int t = 0; t < 5; ++t)
      acc[t] = __builtin_amdgcn_mfma_f32_16x16x32_bf16(__builtin_bit_cast(bf16x8, a1), __builtin_bit_cast(bf16x8, __builtin_shufflevector(bb[t][2], bb[t][3], 0, 1, 2, 3, 4, 5, 6, 7)), acc[t], 0, 0, 0);
    SB();
    st_quad(rws, offA, quad_of_lanes(sel4(lr, acc[0][0], acc[1][0], acc[2][0], acc[3][0]), tag));
    if (w2 == 0) st_quad(rws, offB, quad_of_lanes(acc[4][0], tag));
    add_flag(FL_FIN);
  } else if (wave == 10) {
    __builtin_amdgcn_s_setprio(1);
    const int r = j >> 2, c4 = j & 3;
    const rsrc_t rw = mk(a.wd, F * D * 2);
    const rsrc_t rws = mk(a.ws, WS_END);
    u32 vo[21];
#pragma unroll
    for (int i = 0; i < 21; ++i) {
      const u32 id = i * 64 + lane;
      const u32 row = (id * 3641u) >> 16, c = id - row * 18;
      const u32 grow = row >= 64 ? 4096 + 8 * r + (row - 64) : 64 * r + row;
      vo[i] = id < 1296u ? grow * 1152 + c4 * 288 + c * 16 : OOB;
      asm volatile("" : "+v"(vo[i]));
    }
    const Tag tag = gettag();
    wait_flag(FL_G0, 3);
    wait_flag(FL_MX, 4);
#pragma unroll
    for (int i = 0; i < 21; ++i) dma16(rw, vo[i], 0, L_K + i * 1024);
    __builtin_amdgcn_s_setprio(3);
    VMCNT(DH_LEFT);
    *flg(FL_DH) = 1u;
    VMCNT(0);
    set_flag(FL_DL);
    down_gemv(0, j, lane, tag, rws);
  } else {
    __builtin_amdgcn_s_setprio(3);
    const rsrc_t rws = mk(a.ws, WS_END);
    const int h = j >> 3, hf = (j >> 2) & 1;
    const Tag tag = gettag();
    const u32 lr = lane >> 4;
    {
      const u32 base = WS_Q + (j & ~15) * 256 + lane * 16;
      wait_flag(FL_QPUB, 1);
      __builtin_amdgcn_s_sleep(Q_DELAY);
      wait_flag(FL_VH, 1);
      gather<4>(rws, tag, [&](int i) { return base + i * 1024; }, [&](const v4u (&g)[4]) {
        f32x4 v[4];
#pragma unroll
        for (int i = 0; i < 4; ++i) v[i] = gv4(g[i]);
        float qv[4];
#pragma unroll
        for (int k = 0; k < 4; ++k) qv[k] = swap16_add(swap32_add(v[0][k], v[2][k]), swap32_add(v[1][k], v[3][k]));
        *(LAS v2u*)(smem + L_QI + (lr * 64 + (lane & 15) * 4) * 2) = v2u{pk(qv[0], qv[1]), pk(qv[2], qv[3])};
        set_flag(FL_Q);
      });
    }
    {
      const u32 bo = WS_OP + ((h * 16 + lr) * 32 + hf * 16 + (lane & 15)) * 16;
      const u32 bl = WS_ML + (h * 16 + ((lane & 12) | ((lane + lr) & 3))) * 16;
      wait_flag(FL_OPUB, 1);
      __builtin_amdgcn_s_sleep(O_DELAY);
      wait_flag(FL_DH, 1);
      gather<5>(rws, tag, [&](int i) { return i == 0 ? bl : bo + (i - 1) * 2048; }, [&](const v4u (&g)[5]) {
        const float m = gv(g[0].x), l = gv(g[0].y);
        float M = m; M = fmaxf(M, dpp<DPP_XOR1>(M)); M = fmaxf(M, dpp<DPP_XOR2>(M)); M = fmaxf(M, dpp<DPP_HMIRROR>(M)); M = fmaxf(M, dpp<DPP_MIRROR>(M));
        const float wt = __builtin_amdgcn_exp2f(m - M);
        float L = wt * l; L += dpp<DPP_XOR1>(L); L += dpp<DPP_XOR2>(L); L += dpp<DPP_HMIRROR>(L); L += dpp<DPP_MIRROR>(L);
        f32x4 acc = dpp<0x150>(wt) * gv4(g[1]);
        acc += dpp<0x154>(wt) * gv4(g[2]);
        acc += dpp<0x158>(wt) * gv4(g[3]);
        acc += dpp<0x15C>(wt) * gv4(g[4]);
        const float o = swap16_add(swap32_add(acc[0], acc[2]), swap32_add(acc[1], acc[3])) * __builtin_amdgcn_rcpf(L);
        ((LAS bf16_t*)(smem + L_OI))[(lane & 15) * 4 + lr] = (bf16_t)(pk(o, o) & 0xFFFFu);
        set_flag(FL_O);
      });
    }
  }
}

}

extern "C" void kernel_launch(void* const* d_in, const int* in_sizes, int n_in, void* d_out, int out_size, void* d_ws, size_t ws_size,
                              hipStream_t stream) {
  Args a;
  a.x = (const bf16_t*)d_in[0]; a.kc = (const bf16_t*)d_in[1]; a.vc = (const bf16_t*)d_in[2];
  a.wq = (const bf16_t*)d_in[3]; a.wo = (const bf16_t*)d_in[4];
  a.wg = (const bf16_t*)d_in[6]; a.wu = (const bf16_t*)d_in[7]; a.wd = (const bf16_t*)d_in[8];
  a.out = (bf16_t*)d_out; a.ws = (unsigned char*)d_ws;
  (void)hipFuncSetAttribute((const void*)fused_kernel, hipFuncAttributeMaxDynamicSharedMemorySize, L_END);
  fused_kernel<<<256, NTHR, L_END, stream>>>(a);
}
```

```cpp
#include <hip/hip_runtime.h>
#include <hip/hip_bf16.h>
#include <stdint.h>

typedef unsigned short bf16_t;
typedef unsigned v4u __attribute__((ext_vector_type(4)));

constexpr int D = 576, HQ = 32, HKV = 16, SK = 65536, DH = 128, W = 4096, F = 4608;
constexpr int NQ = HQ * DH;

__device__ __forceinline__ float bf2f(unsigned short b) { return __uint_as_float(((unsigned)b) << 16); }
__device__ __forceinline__ unsigned short f2bf(float f) {
  unsigned u = __float_as_uint(f);
  u += 0x7FFFu + ((u >> 16) & 1u);
  return (unsigned short)(u >> 16);
}
__device__ __forceinline__ float rbf(float f) { return bf2f(f2bf(f)); }

__device__ void layernorm_lds(const bf16_t* x, float* xn, float* red) {
  int tid = threadIdx.x;
  if (tid < 64) {
    float v[9]; float s = 0.f;
    for (int i = 0; i < 9; ++i) { v[i] = bf2f(x[tid + 64 * i]); s += v[i]; }
    for (int m = 32; m >= 1; m >>= 1) s += __shfl_xor(s, m);
    float mean = s / 576.f;
    float q = 0.f;
    for (int i = 0; i < 9; ++i) { float d = v[i] - mean; q += d * d; }
    for (int m = 32; m >= 1; m >>= 1) q += __shfl_xor(q, m);
    float var = rbf(q / 576.f);
    float mb = rbf(mean);
    float den = rbf(sqrtf(rbf(var + 1e-5f)));
    for (int i = 0; i < 9; ++i) xn[tid + 64 * i] = rbf(rbf(v[i] - mb) / den);
  }
  __syncthreads();
}

__global__ void __launch_bounds__(256) k1(const bf16_t* x, const bf16_t* wq, const bf16_t* wg, const bf16_t* wu, float* q, float* g, float* u) {
  __shared__ float xn[D]; __shared__ float red[256 * 8];
  layernorm_lds(x, xn, red);
  int b = blockIdx.x; const bf16_t* Wm; float* out; int N; int c0;
  if (b < 64) { Wm = wq; out = q; N = NQ; c0 = b * 64; }
  else if (b < 64 + 72) { Wm = wg; out = g; N = F; c0 = (b - 64) * 64; }
  else { Wm = wu; out = u; N = F; c0 = (b - 136) * 64; }
  int tid = threadIdx.x; int cc = tid & 7, r0 = tid >> 3;
  float acc[8] = {0, 0, 0, 0, 0, 0, 0, 0};
  for (int r = r0; r < D; r += 32) {
    v4u w = *(const v4u*)(Wm + (size_t)r * N + c0 + cc * 8);
    float xv = xn[r];
    for (int k = 0; k < 4; ++k) { acc[2 * k] += xv * __uint_as_float(w[k] << 16); acc[2 * k + 1] += xv * __uint_as_float(w[k] & 0xffff0000u); }
  }
  for (int k = 0; k < 8; ++k) red[tid * 8 + k] = acc[k];
  __syncthreads();
  if (tid < 64) { float s = 0.f; int c = tid >> 3, k = tid & 7; for (int r = 0; r < 32; ++r) s += red[(r * 8 + c) * 8 + k]; out[c0 + tid] = rbf(s); }
}

__global__ void __launch_bounds__(256) k2(const float* q, const bf16_t* kc, const bf16_t* vc, float* pm, float* pl, float* po) {
  __shared__ float sc[2][256]; __shared__ float red[16][2][128]; __shared__ float mm[2], ll[2];
  int s = blockIdx.x, h = blockIdx.y; int tid = threadIdx.x; int c = tid & 15, r = tid >> 4;
  const bf16_t* kb = kc + ((size_t)h * SK + (SK - W) + s * 256) * DH; const bf16_t* vb = vc + ((size_t)h * SK + (SK - W) + s * 256) * DH;
  float q0[8], q1[8];
  for (int k = 0; k < 8; ++k) { q0[k] = q[(2 * h) * DH + c * 8 + k]; q1[k] = q[(2 * h + 1) * DH + c * 8 + k]; }
  for (int it = 0; it < 16; ++it) {
    int key = it * 16 + r; v4u w = *(const v4u*)(kb + (size_t)key * DH + c * 8);
    float a0 = 0.f, a1 = 0.f;
    for (int k = 0; k < 4; ++k) { float lo = __uint_as_float(w[k] << 16), hi = __uint_as_float(w[k] & 0xffff0000u); a0 += lo * q0[2 * k] + hi * q0[2 * k + 1]; a1 += lo * q1[2 * k] + hi * q1[2 * k + 1]; }
    for (int m = 8; m >= 1; m >>= 1) { a0 += __shfl_xor(a0, m); a1 += __shfl_xor(a1, m); }
    if (c == 0) { sc[0][key] = a0 * 0.08838834764831845f; sc[1][key] = a1 * 0.08838834764831845f; }
  }
  __syncthreads();
  if (tid < 128) {
    int hh = tid >> 6, l = tid & 63; float m = -INFINITY;
    for (int i = 0; i < 4; ++i) m = fmaxf(m, sc[hh][l + 64 * i]);
    for (int o = 32; o >= 1; o >>= 1) m = fmaxf(m, __shfl_xor(m, o));
    float sum = 0.f;
    for (int i = 0; i < 4; ++i) { float p = __expf(sc[hh][l + 64 * i] - m); sc[hh][l + 64 * i] = p; sum += p; }
    for (int o = 32; o >= 1; o >>= 1) sum += __shfl_xor(sum, o);
    if (l == 0) { mm[hh] = m; ll[hh] = sum; }
  }
  __syncthreads();
  float a0[8] = {0, 0, 0, 0, 0, 0, 0, 0}, a1[8] = {0, 0, 0, 0, 0, 0, 0, 0};
  for (int it = 0; it < 16; ++it) {
    int key = it * 16 + r; v4u w = *(const v4u*)(vb + (size_t)key * DH + c * 8);
    float p0 = sc[0][key], p1 = sc[1][key];
    for (int k = 0; k < 4; ++k) { float lo = __uint_as_float(w[k] << 16), hi = __uint_as_float(w[k] & 0xffff0000u); a0[2 * k] += p0 * lo; a0[2 * k + 1] += p0 * hi; a1[2 * k] += p1 * lo; a1[2 * k + 1] += p1 * hi; }
  }
  for (int k = 0; k < 8; ++k) { red[r][0][c * 8 + k] = a0[k]; red[r][1][c * 8 + k] = a1[k]; }
  __syncthreads();
  { int hh = tid >> 7, d = tid & 127; float sum = 0.f; for (int i = 0; i < 16; ++i) sum += red[i][hh][d];
    int head = 2 * h + hh; po[((size_t)head * 16 + s) * DH + d] = sum;
    if (d == 0) { pm[head * 16 + s] = mm[hh]; pl[head * 16 + s] = ll[hh]; } }
}

__global__ void __launch_bounds__(576) k3(const float* pm, const float* pl, const float* po, const bf16_t* wo, const float* g, const float* u, const bf16_t* wd, float* sp, float* fp) {
  __shared__ float vec[128]; __shared__ float red[8][576];
  int b = blockIdx.x, tid = threadIdx.x; const bf16_t* Wm; float* out;
  if (b < 32) {
    if (tid < 128) {
      float M = -INFINITY; for (int s = 0; s < 16; ++s) M = fmaxf(M, pm[b * 16 + s]);
      float L = 0.f, o = 0.f; for (int s = 0; s < 16; ++s) { float w = __expf(pm[b * 16 + s] - M); L += w * pl[b * 16 + s]; o += w * po[((size_t)b * 16 + s) * DH + tid]; }
      vec[tid] = rbf(o / L);
    }
    Wm = wo + (size_t)b * 128 * D; out = sp + b * D;
  } else {
    int bb = b - 32;
    if (tid < 128) { float gv = g[bb * 128 + tid], uv = u[bb * 128 + tid]; float sl = rbf(gv / (1.f + __expf(-gv))); vec[tid] = rbf(sl * uv); }
    Wm = wd + (size_t)bb * 128 * D; out = fp + bb * D;
  }
  __syncthreads();
  int cc = tid % 72, r0 = tid / 72; float acc[8] = {0, 0, 0, 0, 0, 0, 0, 0};
  for (int r = r0; r < 128; r += 8) {
    v4u w = *(const v4u*)(Wm + (size_t)r * D + cc * 8); float xv = vec[r];
    for (int k = 0; k < 4; ++k) { acc[2 * k] += xv * __uint_as_float(w[k] << 16); acc[2 * k + 1] += xv * __uint_as_float(w[k] & 0xffff0000u); }
  }
  for (int k = 0; k < 8; ++k) red[r0][cc * 8 + k] = acc[k];
  __syncthreads();
  float s = 0.f; for (int r = 0; r < 8; ++r) s += red[r][tid];
  out[tid] = s;
}

__global__ void __launch_bounds__(576) k4(const bf16_t* x, const float* sp, const float* fp, bf16_t* out) {
  int tid = threadIdx.x; float s = 0.f, f = 0.f;
  for (int i = 0; i < 32; ++i) s += sp[i * D + tid];
  for (int i = 0; i < 36; ++i) f += fp[i * D + tid];
  s = rbf(s); f = rbf(f); float g = rbf(f / (1.f + __expf(-f)));
  out[tid] = f2bf(bf2f(x[tid]) + rbf(s * g));
}

__global__ void __launch_bounds__(64, 1) k0(float* p) {  if (threadIdx.x == 9999) p[0] = 1.f; }
extern "C" void kernel_launch(void* const* d_in, const int* in_sizes, int n_in, void* d_out, int out_size, void* d_ws, size_t ws_size, hipStream_t stream) {
  const bf16_t* x = (const bf16_t*)d_in[0]; const bf16_t* kc = (const bf16_t*)d_in[1]; const bf16_t* vc = (const bf16_t*)d_in[2];
  const bf16_t* wq = (const bf16_t*)d_in[3]; const bf16_t* wo = (const bf16_t*)d_in[4];
  const bf16_t* wg = (const bf16_t*)d_in[6]; const bf16_t* wu = (const bf16_t*)d_in[7]; const bf16_t* wd = (const bf16_t*)d_in[8];
  float* ws = (float*)d_ws;
  float* q = ws; float* g = q + 4096; float* u = g + 4608; float* pm = u + 4608; float* pl = pm + 512; float* po = pl + 512; float* sp = po + 32 * 16 * 128; float* fp = sp + 32 * 576;
  (void)hipFuncSetAttribute((const void*)k0, hipFuncAttributeMaxDynamicSharedMemorySize, 0);
  k0<<<1, 64, 0, stream>>>(ws);
  k1<<<208, 256, 0, stream>>>(x, wq, wg, wu, q, g, u);
  k2<<<dim3(16, 16), 256, 0, stream>>>(q, kc, vc, pm, pl, po);
  k3<<<68, 576, 0, stream>>>(pm, pl, po, wo, g, u, wd, sp, fp);
  k4<<<1, 576, 0, stream>>>(x, sp, fp, (bf16_t*)d_out);
}
```

```cpp
#include <hip/hip_runtime.h>
#include <stdint.h>

typedef unsigned short bf16_t;
typedef unsigned u32;
typedef unsigned long long u64;
typedef u32 v4u __attribute__((ext_vector_type(4)));
typedef u32 v2u __attribute__((ext_vector_type(2)));
typedef float f32x4 __attribute__((ext_vector_type(4)));
typedef short s16x4 __attribute__((ext_vector_type(4)));
typedef short s16x8 __attribute__((ext_vector_type(8)));
typedef __bf16 bf16x2 __attribute__((ext_vector_type(2)));
typedef __bf16 bf16x8 __attribute__((ext_vector_type(8)));
#define LAS __attribute__((address_space(3)))
#define GAS __attribute__((address_space(1)))
typedef __amdgpu_buffer_rsrc_t rsrc_t;

namespace {

constexpr int D = 576, SK = 65536, DH = 128, WIN = 4096, F = 4608, NQ = 4096;
constexpr int NWAVES = 12, NTHR = NWAVES * 64;

constexpr u32 WS_EPOCH = 0;
constexpr u32 WS_Q = 4096;
constexpr u32 WS_GM = WS_Q + 256 * 256;
constexpr u32 WS_GP = WS_GM + 64 * 8 * 256;
constexpr u32 WS_OP = WS_GP + 256 * 256;
constexpr u32 WS_ML = WS_OP + 32 * 16 * 512;
constexpr u32 WS_FIN = WS_ML + 32 * 16 * 16;
constexpr u32 WS_END = WS_FIN + 72 * 2 * 64 * 32;

constexpr u32 L_K = 0;
constexpr u32 L_V = 65536;
constexpr u32 L_WO = 131072;
constexpr u32 L_XN = L_WO + 18432;
constexpr u32 L_QI = L_XN + 2304;
constexpr u32 L_PI = L_QI + 512;
constexpr u32 L_OW = L_PI + 1024;
constexpr u32 L_HI = L_OW + 4096;
constexpr u32 L_OI = L_HI + 160;
constexpr u32 L_MX = L_OI + 128;
constexpr u32 L_TAG = L_MX + 64;
constexpr u32 L_FLG = L_TAG + 32;
constexpr u32 L_END = L_FLG + 128;
static_assert(L_END <= 163840, "LDS");
enum { FL_Q, FL_VISS, FL_MX, FL_OW, FL_OPUB, FL_H, FL_O, FL_WOL, FL_FIN, FL_G0, FL_TAG, FL_W0, FL_M2, FL_QPUB, FL_HPUB, FL_DL, FL_VH, FL_DH };

constexpr int AUX_NT = 2, AUX_SC1 = 16;
constexpr u32 SPIN_CAP = 1u << 20;
constexpr u32 OOB = 0x7FFFFFF0u;

#define Q_DELAY 4
#define H_DELAY 3
#define O_DELAY 3
#define F_DELAY 6
#define VH_AT 10
#define DH_LEFT 8
#define KVTILE(t, s) ((t) * 16 + (((s) + (t)) & 15))

constexpr int RSHIFT = 32;
#define RBLK(j) ((((j) >> 2) + RSHIFT) & 63)

extern __shared__ __attribute__((aligned(16))) unsigned char smem[];

__device__ __forceinline__ float lo2f(u32 p) { return __uint_as_float(p << 16); }
__device__ __forceinline__ float hi2f(u32 p) { return __uint_as_float(p & 0xFFFF0000u); }
__device__ __forceinline__ u32 pk(float a, float b) {
  bf16x2 r = {(__bf16)a, (__bf16)b};
  return __builtin_bit_cast(u32, r);
}
__device__ __forceinline__ float rbf(float f) { return lo2f(pk(f, f)); }
__device__ __forceinline__ float sel4(u32 g, float a, float b, float c, float d) {
  const bool b0 = g & 1, b1 = g & 2;
  const float lo = b0 ? b : a, hi = b0 ? d : c;
  return b1 ? hi : lo;
}
__device__ __forceinline__ rsrc_t mk(const void* p, u32 bytes) {
  return __builtin_amdgcn_make_buffer_rsrc((void*)p, 0, bytes, 0x00020000);
}
__device__ __forceinline__ float dot2(u32 w, u32 x, float acc) {
  return __builtin_amdgcn_fdot2_f32_bf16(__builtin_bit_cast(bf16x2, w), __builtin_bit_cast(bf16x2, x), acc, false);
}
template <int CTRL> __device__ __forceinline__ float dpp(float x) {
  return __builtin_bit_cast(float, __builtin_amdgcn_mov_dpp(__builtin_bit_cast(int, x), CTRL, 0xf, 0xf, true));
}
constexpr int DPP_XOR1 = 0xB1, DPP_XOR2 = 0x4E, DPP_HMIRROR = 0x141, DPP_MIRROR = 0x140, DPP_ROR4 = 0x124, DPP_ROR8 = 0x128;
__device__ __forceinline__ float swap16_add(float a, float b) {
  auto s = __builtin_amdgcn_permlane16_swap(__float_as_uint(a), __float_as_uint(b), false, false);
  return __uint_as_float(s[0]) + __uint_as_float(s[1]);
}
__device__ __forceinline__ float swap32_add(float a, float b) {
  auto s = __builtin_amdgcn_permlane32_swap(__float_as_uint(a), __float_as_uint(b), false, false);
  return __uint_as_float(s[0]) + __uint_as_float(s[1]);
}
__device__ __forceinline__ float wave_sum(float x) {
  x += dpp<DPP_XOR1>(x); x += dpp<DPP_XOR2>(x); x += dpp<DPP_HMIRROR>(x); x += dpp<DPP_MIRROR>(x);
  x = swap16_add(x, x); x = swap32_add(x, x);
  return x;
}
__device__ __forceinline__ float wave_max(float x) {
  x = fmaxf(x, dpp<DPP_XOR1>(x)); x = fmaxf(x, dpp<DPP_XOR2>(x)); x = fmaxf(x, dpp<DPP_HMIRROR>(x)); x = fmaxf(x, dpp<DPP_MIRROR>(x));
  auto s = __builtin_amdgcn_permlane16_swap(__float_as_uint(x), __float_as_uint(x), false, false);
  x = fmaxf(__uint_as_float(s[0]), __uint_as_float(s[1]));
  auto t = __builtin_amdgcn_permlane32_swap(__float_as_uint(x), __float_as_uint(x), false, false);
  return fmaxf(__uint_as_float(t[0]), __uint_as_float(t[1]));
}
__device__ __forceinline__ float silu_b(float v) {
  return rbf(v * __builtin_amdgcn_rcpf(1.f + __builtin_amdgcn_exp2f(-1.4426950408889634f * v)));
}

struct Tag { u32 t0, t1, t2, t3, vm; };
__device__ __forceinline__ u32 q22r(u32 rounded, u32 vm, u32 t) { return __builtin_amdgcn_bitop3_b32(rounded, vm, t, 0xEA); }
__device__ __forceinline__ u32 rnd22(float v) { return __float_as_uint(v) + 0x200u; }
__device__ __forceinline__ float r22(float v) { return __uint_as_float(rnd22(v) & 0xFFFFFC00u); }
__device__ __forceinline__ v4u quad(float a, float b, float c, float d, Tag t) {
  return v4u{q22r(rnd22(a), t.vm, t.t0), q22r(rnd22(b), t.vm, t.t1), q22r(rnd22(c), t.vm, t.t2), q22r(rnd22(d), t.vm, t.t3)};
}
__device__ __forceinline__ v4u quad_of_lanes(float v, Tag tag) {
  const float x = __uint_as_float(rnd22(v));
  return v4u{q22r(__float_as_uint(dpp<0x00>(x)), tag.vm, tag.t0), q22r(__float_as_uint(dpp<0x55>(x)), tag.vm, tag.t1),
             q22r(__float_as_uint(dpp<0xAA>(x)), tag.vm, tag.t2), q22r(__float_as_uint(dpp<0xFF>(x)), tag.vm, tag.t3)};
}
__device__ __forceinline__ float gv(u32 d) { return __uint_as_float(d & 0xFFFFFC00u); }
__device__ __forceinline__ f32x4 gv4(v4u v) { return f32x4{gv(v.x), gv(v.y), gv(v.z), gv(v.w)}; }
__device__ __forceinline__ void st_quad(rsrc_t rs, u32 off, v4u q) { __builtin_amdgcn_raw_buffer_store_b128(q, rs, off, 0, AUX_SC1); }
template <int N, class Off, class Use>
__device__ __forceinline__ void gather(rsrc_t rws, Tag tag, Off off, Use use) {
  v4u a[N];
#pragma unroll 1
  for (u32 sp = 0; sp < SPIN_CAP; ++sp) {
    asm volatile("" ::: "memory");
#pragma unroll
    for (int i = 0; i < N; ++i) a[i] = __builtin_amdgcn_raw_buffer_load_b128(rws, off(i), 0, AUX_SC1);
    u32 bad = 0;
#pragma unroll
    for (int i = 0; i < N; ++i) {
      bad = __builtin_amdgcn_bitop3_b32(a[i].x, tag.t0, bad, 0xBE); bad = __builtin_amdgcn_bitop3_b32(a[i].y, tag.t1, bad, 0xBE);
      bad = __builtin_amdgcn_bitop3_b32(a[i].z, tag.t2, bad, 0xBE); bad = __builtin_amdgcn_bitop3_b32(a[i].w, tag.t3, bad, 0xBE);
    }
    if (__all((bad & 0x3FFu) == 0u)) break;
  }
  use(a);
}

__device__ __forceinline__ volatile LAS u32* flg(int i) { return (volatile LAS u32*)(smem + L_FLG) + i; }
__device__ __forceinline__ void wait_flag(int i, u32 want) {
  volatile LAS u32* p = flg(i);
#pragma unroll 1
  for (u32 s = 0; s < SPIN_CAP; ++s) {
    if (__builtin_amdgcn_readfirstlane(*p) >= want) break;
    __builtin_amdgcn_s_sleep(1);
  }
  asm volatile("" ::: "memory");
}
__device__ __forceinline__ void wait_flag_tight(int i, u32 want) {
  volatile LAS u32* p = flg(i);
#pragma unroll 1
  for (u32 s = 0; s < SPIN_CAP; ++s) {
    if (__builtin_amdgcn_readfirstlane(*p) >= want) break;
  }
  asm volatile("" ::: "memory");
}
__device__ __forceinline__ void set_flag(int i) {
  asm volatile("s_waitcnt lgkmcnt(0)" ::: "memory");
  *flg(i) = 1u;
  asm volatile("" ::: "memory");
}
__device__ __forceinline__ void add_flag(int i) {
  asm volatile("s_waitcnt lgkmcnt(0)" ::: "memory");
  if ((threadIdx.x & 63) == 0) __hip_atomic_fetch_add((LAS u32*)(smem + L_FLG) + i, 1u, __ATOMIC_RELAXED, __HIP_MEMORY_SCOPE_WORKGROUP);
  asm volatile("" ::: "memory");
}
__device__ __forceinline__ u32 bump_flag(int i) {
  asm volatile("s_waitcnt lgkmcnt(0)" ::: "memory");
  u32 r = 0;
  if ((threadIdx.x & 63) == 0) r = __hip_atomic_fetch_add((LAS u32*)(smem + L_FLG) + i, 1u, __ATOMIC_RELAXED, __HIP_MEMORY_SCOPE_WORKGROUP);
  r = __builtin_amdgcn_readfirstlane(r);
  asm volatile("" ::: "memory");
  return r;
}
__device__ __forceinline__ Tag gettag() {
  wait_flag(FL_TAG, 1);
  const v4u tg = *(const LAS v4u*)(smem + L_TAG);
  Tag t{(u32)__builtin_amdgcn_readfirstlane(tg.x), (u32)__builtin_amdgcn_readfirstlane(tg.y), (u32)__builtin_amdgcn_readfirstlane(tg.z),
        (u32)__builtin_amdgcn_readfirstlane(tg.w), 0xFFFFFC00u};
  asm volatile("" : "+s"(t.t0), "+s"(t.t1), "+s"(t.t2), "+s"(t.t3), "+v"(t.vm));
  return t;
}
__device__ __forceinline__ void dma16(rsrc_t rs, u32 voff, u32 soff, u32 ldsoff) {
  asm volatile("s_mov_b32 m0, %0\n\ts_nop 0\n\tbuffer_load_dwordx4 %1, %2, %3 offen nt lds" ::"s"(ldsoff), "v"(voff), "s"(rs), "s"(soff) : "m0", "memory");
}
#define VMCNT_(n) asm volatile("s_waitcnt vmcnt(" #n ")" ::: "memory")
#define VMCNT(n) VMCNT_(n)
#define SB() __builtin_amdgcn_sched_barrier(0)

struct Args {
  const bf16_t* x; const bf16_t* kc; const bf16_t* vc; const bf16_t* wq; const bf16_t* wo;
  const bf16_t* wg; const bf16_t* wu; const bf16_t* wd; bf16_t* out; unsigned char* ws;
};

__device__ __forceinline__ void down_gemv(int part, int j, int lane, Tag tag, rsrc_t rws) {
  const int r = RBLK(j), c4 = j & 3;
  const u32 lr = lane >> 4, q4 = (lane & 15) >> 2, p4 = lane & 3;
  const u32 badr = L_K + (8 * lr + q4) * 288 + p4 * 8 + part * 96;
  const u32 row4 = 64 + 4 * lr + q4;
  const u32 badr4 = L_K + (row4 < 72 ? row4 : 71) * 288 + p4 * 8 + part * 96;
  const u32 cq = c4 * 36 + (3 * part + lr) * 4 + q4;
  u32 off = (lr < 3 && p4 == 0) ? WS_FIN + (((cq >> 1) * 2 + 1) * 64 + r) * 32 + (cq & 1) * 16 : OOB;
  u32 aadr = L_HI + lr * 16, aadr2 = L_HI + 128 + lr * 8;
  asm volatile("" : "+v"(off), "+v"(aadr), "+v"(aadr2));
  wait_flag(FL_DL, 1);
  s16x4 b[3][5];
#pragma unroll
  for (int t = 0; t < 3; ++t) {
#pragma unroll
    for (int u = 0; u < 4; ++u) b[t][u] = __builtin_amdgcn_ds_read_tr16_b64_v4i16((LAS s16x4*)(smem + badr + ((u >> 1) * 32 + (u & 1) * 4) * 288 + t * 32));
    b[t][4] = __builtin_amdgcn_ds_read_tr16_b64_v4i16((LAS s16x4*)(smem + badr4 + t * 32));
  }
#pragma unroll
  for (int t = 0; t < 3; ++t)
#pragma unroll
    for (int u = 0; u < 5; ++u) asm volatile("" : "+v"(b[t][u]));
  wait_flag_tight(FL_H, 2);
  const s16x8 a0 = *(const LAS s16x8*)(smem + aadr), a1 = *(const LAS s16x8*)(smem + aadr + 64);
  const s16x4 a2 = *(const LAS s16x4*)(smem + aadr2);
  SB();
  f32x4 acc[3];
#pragma unroll
  for (int t = 0; t < 3; ++t)
    acc[t] = __builtin_amdgcn_mfma_f32_16x16x32_bf16(__builtin_bit_cast(bf16x8, a0), __builtin_bit_cast(bf16x8, __builtin_shufflevector(b[t][0], b[t][1], 0, 1, 2, 3, 4, 5, 6, 7)), f32x4{0.f, 0.f, 0.f, 0.f}, 0, 0, 0);
  SB();
#pragma unroll
  for (int t = 0; t < 3; ++t)
    acc[t] = __builtin_amdgcn_mfma_f32_16x16x32_bf16(__builtin_bit_cast(bf16x8, a1), __builtin_bit_cast(bf16x8, __builtin_shufflevector(b[t][2], b[t][3], 0, 1, 2, 3, 4, 5, 6, 7)), acc[t], 0, 0, 0);
  SB();
#pragma unroll
  for (int t = 0; t < 3; ++t) acc[t] = __builtin_amdgcn_mfma_f32_16x16x16bf16_1k(a2, b[t][4], acc[t], 0, 0, 0);
  SB();
  st_quad(rws, off, quad_of_lanes(sel4(lr, acc[0][0], acc[1][0], acc[2][0], acc[2][0]), tag));
  add_flag(FL_FIN);
}

__global__ void __launch_bounds__(NTHR, 3) fused_kernel(Args a) {
  const int tid = threadIdx.x, lane = tid & 63;
  const int wave = __builtin_amdgcn_readfirstlane(tid >> 6);
  const int b = blockIdx.x;
  const int j = (b & 7) * 32 + (b >> 3);
  if (wave == 11) {
    if (lane < 32) *flg(lane) = 0u;
    if (lane >= 32 && lane < 36) *(LAS u32*)(smem + L_HI + 144 + (lane - 32) * 4) = 0u;
  }
  SB();
  asm volatile("s_waitcnt lgkmcnt(0)" ::: "memory");
  __builtin_amdgcn_s_barrier();
  asm volatile("" ::: "memory");
  SB();

  if (__builtin_expect(wave < 4, 1)) {
    __builtin_amdgcn_s_setprio(3);
    const int kp = j & 3, r = j >> 2;
    const bf16_t* wm; u32 row0, stride, strip, nrows, pub;
    if (wave == 0) { wm = a.wq; row0 = 144 * kp; stride = NQ * 2; strip = r; nrows = 144; pub = WS_Q + j * 256; }
    else if (wave < 3) {
      const int quarter = 2 * (kp & 1) + (wave - 1);
      wm = (kp >> 1) ? a.wu : a.wg; row0 = 144 * quarter; stride = F * 2; nrows = 144;
      strip = (r & ~7) | ((r + 3 * wave) & 7);
      pub = WS_GM + ((strip * 2 + (kp >> 1)) * 4 + quarter) * 256;
    } else { wm = ((j >> 4) & 1) ? a.wu : a.wg; row0 = 36 * (j & 15); stride = F * 2; strip = 64 + (j >> 5); nrows = 36; pub = WS_GP + j * 256; }
    const rsrc_t rw = mk((const unsigned char*)wm + (size_t)row0 * stride + strip * 128, (nrows - 1) * stride + 128);
    const rsrc_t rx = mk(a.x, D * 2);
    const rsrc_t rws = mk(a.ws, WS_END);
    u64 tg = __hip_atomic_load((GAS u64*)(a.ws + WS_EPOCH), __ATOMIC_RELAXED, __HIP_MEMORY_SCOPE_AGENT);
    u32 xr[5];
#pragma unroll
    for (int i = 0; i < 5; ++i) xr[i] = __builtin_amdgcn_raw_buffer_load_b32(rx, lane * 4, i * 256, 0);
    const u32 rs8 = lane >> 3;
    const u32 voff = rs8 * stride + (lane & 7) * 16;
    Tag tag;
    auto layernorm = [&](v4u (&av)[18]) {
#pragma unroll
      for (int i = 0; i < 5; ++i) asm volatile("" : "+v"(xr[i]));
      asm volatile("" : "+v"(tg));
      {
        tg += 1;
        const u64 T = ((u64)(u32)__builtin_amdgcn_readfirstlane((u32)(tg >> 32)) << 32) | (u32)__builtin_amdgcn_readfirstlane((u32)tg);
        const u64 H = T * 0x9E3779B97F4A7C15ull;
        tag = Tag{(u32)T & 0x3FFu, (u32)(H >> 54), (u32)(H >> 44) & 0x3FFu, (u32)(H >> 34) & 0x3FFu, 0xFFFFFC00u};
        asm volatile("" : "+v"(tag.vm));
        if (wave == 0) {
          if (lane == 0) { *(LAS v4u*)(smem + L_TAG) = v4u{tag.t0, tag.t1, tag.t2, tag.t3}; *(LAS v2u*)(smem + L_TAG + 16) = v2u{(u32)T, (u32)(T >> 32)}; }
          set_flag(FL_TAG);
        }
      }
      float s = 0.f;
#pragma unroll
      for (int i = 0; i < 5; ++i) s = dot2(xr[i], 0x3F803F80u, s);
      const float mean = wave_sum(s) * (1.f / 576.f);
      float q = 0.f, q4 = 0.f;
#pragma unroll
      for (int i = 0; i < 4; ++i) { const float d0 = lo2f(xr[i]) - mean, d1 = hi2f(xr[i]) - mean; q = fmaf(d0, d0, q); q = fmaf(d1, d1, q); }
      { const float d0 = lo2f(xr[4]) - mean, d1 = hi2f(xr[4]) - mean; q4 = fmaf(d0, d0, d1 * d1); }
      q += lane < 32 ? q4 : 0.f;
      const float var = rbf(wave_sum(q) * (1.f / 576.f));
      const float mb = rbf(mean);
      const float inv = __builtin_amdgcn_rcpf(rbf(__builtin_amdgcn_sqrtf(rbf(var + 1e-5f))));
      LAS v2u* xn = (LAS v2u*)(smem + L_XN);
#pragma unroll
      for (int i = 0; i < 5; ++i) {
        const u32 d = pk(lo2f(xr[i]) - mb, hi2f(xr[i]) - mb);
        const u32 n = pk(lo2f(d) * inv, hi2f(d) * inv);
        if (i < 4 || lane < 32) xn[i * 64 + lane] = v2u{__builtin_amdgcn_perm(n, n, 0x01000100u), __builtin_amdgcn_perm(n, n, 0x03020302u)};
      }
      asm volatile("s_waitcnt lgkmcnt(0)" ::: "memory");
      u32 xp[18];
      const LAS u32* xs = (const LAS u32*)(smem + L_XN) + row0 + rs8;
#pragma unroll
      for (int i = 0; i < 18; ++i) xp[i] = xs[(wave == 3 && i > 4) ? 0 : 8 * i];
      if (wave == 3 && rs8 >= 4) xp[4] = 0u;
      u32 msk[4];
#pragma unroll
      for (int d = 0; d < 4; ++d) msk[d] = (((lane & 7) >> 1) == d) ? ((lane & 1) ? 0xFFFF0000u : 0x0000FFFFu) : 0u;
#pragma unroll
      for (int i = 0; i < 18; ++i) av[i] = v4u{xp[i] & msk[0], xp[i] & msk[1], xp[i] & msk[2], xp[i] & msk[3]};
    };
    auto gemv_publish = [&](const v4u (&av)[18], const v4u (&w)[18]) {
      f32x4 da = {0.f, 0.f, 0.f, 0.f}, db = {0.f, 0.f, 0.f, 0.f};
#pragma unroll
      for (int i = 0; i < 18; ++i) {
        if (i & 1) db = __builtin_amdgcn_mfma_f32_16x16x32_bf16(__builtin_bit_cast(bf16x8, av[i]), __builtin_bit_cast(bf16x8, w[i]), db, 0, 0, 0);
        else da = __builtin_amdgcn_mfma_f32_16x16x32_bf16(__builtin_bit_cast(bf16x8, av[i]), __builtin_bit_cast(bf16x8, w[i]), da, 0, 0, 0);
      }
      da += db;
      f32x4 res;
#pragma unroll
      for (int k = 0; k < 4; ++k) {
        const float up = dpp<DPP_ROR8>(da[k]);
        res[k] = da[k] + __uint_as_float(__builtin_amdgcn_permlane32_swap(__float_as_uint(up), __float_as_uint(up), false, false)[1]);
      }
      const u32 off = ((lane & 0x28) == 0) ? pub + (2 * (lane & 7) + (lane >> 4)) * 16 : OOB;
      st_quad(rws, off, quad(res[0], res[1], res[2], res[3], tag));
    };
    if (__builtin_expect(wave == 0, 1)) {
      v4u w[18], av[18];
#pragma unroll
      for (int i = 0; i < 18; ++i) w[i] = __builtin_amdgcn_raw_buffer_load_b128(rw, voff, 8 * i * stride, AUX_NT);
      SB();
      set_flag(FL_W0);
      layernorm(av);
      gemv_publish(av, w);
      set_flag(FL_QPUB);
      {
        const u32 bm = WS_GM + RBLK(j) * 2048 + lane * 16;
        wait_flag(FL_HPUB, 3);
        __builtin_amdgcn_s_sleep(H_DELAY);
        wait_flag(FL_DH, 1);
        gather<2>(rws, tag, [&](int i) { return bm + i * 1024; }, [&](const v4u (&g)[2]) {
          const f32x4 gg = gv4(g[0]), uu = gv4(g[1]);
          const float gs = swap16_add(swap32_add(gg[0], gg[2]), swap32_add(gg[1], gg[3]));
          const float us = swap16_add(swap32_add(uu[0], uu[2]), swap32_add(uu[1], uu[3]));
          const float hv = silu_b(rbf(gs)) * rbf(us);
          ((LAS bf16_t*)(smem + L_HI))[(lane & 15) * 4 + (lane >> 4)] = (bf16_t)(pk(hv, hv) & 0xFFFFu);
          add_flag(FL_H);
        });
      }
      if ((j & 31) < 9) {
        const int f = (j >> 5) * 9 + (j & 31);
        const u32 mycol = f * 8 + 4 * (lane & 1) + 2 * ((lane >> 4) & 1) + ((lane >> 3) & 1);
        const u32 xo = __builtin_amdgcn_raw_buffer_load_b16(rx, mycol * 2, 0, 0);
        const u32 base = WS_FIN + f * 4096 + lane * 16;
        wait_flag(FL_FIN, 5);
        __builtin_amdgcn_s_sleep(F_DELAY);
        gather<4>(rws, tag, [&](int i) { return base + i * 1024; }, [&](const v4u (&g)[4]) {
          const f32x4 sv = gv4(g[0]) + gv4(g[1]), fv = gv4(g[2]) + gv4(g[3]);
          float a4[4];
#pragma unroll
          for (int k = 0; k < 4; ++k) a4[k] = swap32_add(sv[k], fv[k]);
          const float a0 = swap16_add(a4[0], a4[2]), a1 = swap16_add(a4[1], a4[3]);
          float b0 = a0 + dpp<DPP_XOR2>(a0), b1 = a1 + dpp<DPP_XOR2>(a1);
          b0 += dpp<DPP_ROR4>(b0); b1 += dpp<DPP_ROR4>(b1);
          b0 += dpp<DPP_ROR8>(b0); b1 += dpp<DPP_ROR8>(b1);
          const float t = (lane & 8) ? b1 : b0;
          const auto sf = __builtin_amdgcn_permlane32_swap(__float_as_uint(t), __float_as_uint(t), false, false);
          const float sb = rbf(__uint_as_float(sf[0])), fb = rbf(__uint_as_float(sf[1]));
          const float o = lo2f(xo) + rbf(sb * silu_b(fb));
          if ((lane & 0x26) == 0) *(GAS bf16_t*)((GAS unsigned char*)a.out + mycol * 2) = (bf16_t)(pk(o, o) & 0xFFFFu);
        });
        if (f == 0 && lane == 0) {
          const v2u nx = *(const LAS v2u*)(smem + L_TAG + 16);
          __hip_atomic_store((GAS u64*)(a.ws + WS_EPOCH), (u64)nx.x | ((u64)nx.y << 32), __ATOMIC_RELAXED, __HIP_MEMORY_SCOPE_AGENT);
        }
      }
    } else {
      v4u w[18], av[18];
      layernorm(av);
      __builtin_amdgcn_s_setprio(1);
#pragma unroll
      for (int i = 0; i < 18; ++i) asm volatile("" : "+v"(av[i]));
      wait_flag(FL_VISS, 4);
#pragma unroll
      for (int i = 0; i < 18; ++i) w[i] = __builtin_amdgcn_raw_buffer_load_b128(rw, voff, 8 * i * stride, AUX_NT);
      SB();
      add_flag(FL_G0);
      __builtin_amdgcn_s_setprio(3);
      gemv_publish(av, w);
      add_flag(FL_HPUB);
      if (wave == 1) {
        const u32 rb = RBLK(j), bp = WS_GP + ((rb >> 3) * 32 + (lane >> 1)) * 256 + ((rb & 7) * 2 + (lane & 1)) * 16;
        wait_flag(FL_HPUB, 3);
        __builtin_amdgcn_s_sleep(H_DELAY);
        wait_flag(FL_DH, 1);
        gather<1>(rws, tag, [&](int) { return bp; }, [&](const v4u (&g)[1]) {
          f32x4 t = gv4(g[0]);
#pragma unroll
          for (int k = 0; k < 4; ++k) t[k] += dpp<DPP_XOR2>(t[k]);
          float a0 = swap16_add(t[0], t[2]), a1 = swap16_add(t[1], t[3]);
          a0 += dpp<DPP_ROR4>(a0); a1 += dpp<DPP_ROR4>(a1);
          a0 += dpp<DPP_ROR8>(a0); a1 += dpp<DPP_ROR8>(a1);
          const float v = (lane & 8) ? a1 : a0;
          const auto gu = __builtin_amdgcn_permlane32_swap(__float_as_uint(v), __float_as_uint(v), false, false);
          const float hv = silu_b(rbf(__uint_as_float(gu[0]))) * rbf(__uint_as_float(gu[1]));
          if ((lane & 0x26) == 0) ((LAS bf16_t*)(smem + L_HI))[64 + 4 * (lane & 1) + 2 * ((lane >> 4) & 1) + ((lane >> 3) & 1)] = (bf16_t)(pk(hv, hv) & 0xFFFFu);
          add_flag(FL_H);
        });
      } else {
        down_gemv(wave - 1, j, lane, tag, rws);
      }
    }
  } else if (__builtin_expect(wave < 8, 1)) {
    __builtin_amdgcn_s_setprio(0);
    const int w = wave - 4, g = j >> 4, s = j & 15;
    const rsrc_t rk = mk(a.kc + ((size_t)g * SK + (SK - WIN)) * DH, WIN * DH * 2);
    const rsrc_t rv = mk(a.vc + ((size_t)g * SK + (SK - WIN)) * DH, WIN * DH * 2);
    const rsrc_t rws = mk(a.ws, WS_END);
    const u32 lr = lane >> 4;
    u32 vo[4];
#pragma unroll
    for (int sub = 0; sub < 4; ++sub) { vo[sub] = sub * 1024 + lr * 256 + ((((lane & 15) ^ (lr << 2)) ^ sub) << 4); asm volatile("" : "+v"(vo[sub])); }
    wait_flag(FL_W0, 1);
#pragma unroll
    for (int p = 0; p < 16; ++p) dma16(rk, vo[p & 3], KVTILE(4 * w + (p >> 2), s) * 4096, L_K + w * 16384 + p * 1024);
#pragma unroll
    for (int p = 0; p < 16; ++p) {
      dma16(rv, vo[p & 3], KVTILE(4 * w + (p >> 2), s) * 4096, L_V + w * 16384 + p * 1024);
      if (p == VH_AT && w == 0) *flg(FL_VH) = 1u;
    }
    add_flag(FL_VISS);
    __builtin_amdgcn_s_setprio(3);
    const Tag tag = gettag();
    const u32 krow = lane & 15;
    u32 kadr = L_K + w * 16384 + krow * 256 + ((lr ^ (((krow & 3) << 2) | ((krow >> 2) & 3))) << 4);
    const u32 q4 = (lane & 15) >> 2, p4 = lane & 3;
    u32 va[2][8];
#pragma unroll
    for (int t = 0; t < 2; ++t) {
      const u32 row = 8 * (lr & 1) + 4 * t + q4;
      const u32 base = L_V + w * 16384 + (lr >> 1) * 4096 + row * 256 + (((p4 >> 1) ^ ((q4 << 2) | (2 * (lr & 1) + t))) << 4) + 8 * (p4 & 1);
#pragma unroll
      for (int c = 0; c < 8; ++c) { va[t][c] = base ^ (c << 5); asm volatile("" : "+v"(va[t][c])); }
    }
    u32 ob = WS_OP + (((2 * g + (lane >> 5)) * 16 + s) * 32 + (lane & 31)) * 16;
    u32 mlb = (lane & 31) == 0 ? WS_ML + ((2 * g + (lane >> 5)) * 16 + s) * 16 : OOB;
    u32 oadr = L_OW + lane * 16, ladr = L_MX + 32 + (lane >> 5) * 4;
    asm volatile("" : "+v"(kadr), "+v"(ob), "+v"(mlb), "+v"(oadr), "+v"(ladr));
    VMCNT(16);
    s16x8 kf[16];
#pragma unroll
    for (int i = 0; i < 4; ++i)
#pragma unroll
      for (int st = 0; st < 4; ++st) kf[i * 4 + st] = *(const LAS s16x8*)(smem + ((kadr ^ (st << 6)) + i * 4096));
#pragma unroll
    for (int i = 0; i < 16; ++i) asm volatile("" : "+v"(kf[i]));
    add_flag(FL_MX);
    wait_flag(FL_Q, 1);
    s16x8 qf[4];
#pragma unroll
    for (int st = 0; st < 4; ++st) qf[st] = *(const LAS s16x8*)(smem + L_QI + (lane & 1) * 256 + st * 64 + lr * 16);
    f32x4 sc[4];
#pragma unroll
    for (int i = 0; i < 4; ++i) sc[i] = f32x4{0.f, 0.f, 0.f, 0.f};
#pragma unroll
    for (int st = 0; st < 4; ++st)
#pragma unroll
      for (int i = 0; i < 4; ++i)
        sc[i] = __builtin_amdgcn_mfma_f32_16x16x32_bf16(__builtin_bit_cast(bf16x8, qf[st]), __builtin_bit_cast(bf16x8, kf[i * 4 + st]), sc[i], 0, 0, 0);
    constexpr float C2 = 0.08838834764831845f * 1.4426950408889634f;
    const float s0 = sel4(lr, sc[0][0], sc[1][0], sc[2][0], sc[3][0]) * C2;
    const float s1 = sel4(lr, sc[0][1], sc[1][1], sc[2][1], sc[3][1]) * C2;
    LAS float* mx = (LAS float*)(smem + L_MX);
    {
      const float wm0 = wave_max(s0), wm1 = wave_max(s1);
      if (lane == 0) *(LAS v2u*)(mx + 2 * w) = v2u{__float_as_uint(wm0), __float_as_uint(wm1)};
    }
    add_flag(FL_M2);
    wait_flag_tight(FL_M2, 4);
    const f32x4 ma = *(const LAS f32x4*)(mx), mb4 = *(const LAS f32x4*)(mx + 4);
    const float m0 = r22(fmaxf(fmaxf(ma[0], ma[2]), fmaxf(mb4[0], mb4[2])));
    const float m1 = r22(fmaxf(fmaxf(ma[1], ma[3]), fmaxf(mb4[1], mb4[3])));
    const u32 pp = pk(__builtin_amdgcn_exp2f(s0 - m0), __builtin_amdgcn_exp2f(s1 - m1));
    LAS bf16_t* pi = (LAS bf16_t*)(smem + L_PI + w * 256);
    pi[lane] = (bf16_t)(pp & 0xFFFFu); pi[64 + lane] = (bf16_t)(pp >> 16);
    {
      const float l0 = wave_sum(lo2f(pp)), l1 = wave_sum(hi2f(pp));
      if (lane == 0) *(LAS v2u*)(mx + 8 + 2 * w) = v2u{__float_as_uint(l0), __float_as_uint(l1)};
    }
    s16x8 pf[2];
#pragma unroll
    for (int ks = 0; ks < 2; ++ks) pf[ks] = *(const LAS s16x8*)(smem + L_PI + w * 256 + (lane & 1) * 128 + ks * 64 + lr * 16);
    f32x4 oc[8];
#pragma unroll
    for (int c = 0; c < 8; ++c) oc[c] = f32x4{0.f, 0.f, 0.f, 0.f};
#pragma unroll
    for (int ks = 0; ks < 2; ++ks) {
      if (ks == 0) VMCNT(8); else VMCNT(0);
      s16x4 b0[8], b1[8];
#pragma unroll
      for (int c = 0; c < 8; ++c) {
        b0[c] = __builtin_amdgcn_ds_read_tr16_b64_v4i16((LAS s16x4*)(smem + va[0][c] + ks * 8192));
        b1[c] = __builtin_amdgcn_ds_read_tr16_b64_v4i16((LAS s16x4*)(smem + va[1][c] + ks * 8192));
      }
      SB();
#pragma unroll
      for (int c = 0; c < 8; ++c) {
        const s16x8 bb = __builtin_shufflevector(b0[c], b1[c], 0, 1, 2, 3, 4, 5, 6, 7);
        oc[c] = __builtin_amdgcn_mfma_f32_16x16x32_bf16(__builtin_bit_cast(bf16x8, pf[ks]), __builtin_bit_cast(bf16x8, bb), oc[c], 0, 0, 0);
      }
      SB();
    }
    if (lane < 16) {
      LAS float* ow = (LAS float*)(smem + L_OW) + w * 256 + lane;
#pragma unroll
      for (int c = 0; c < 8; ++c) { ow[c * 16] = oc[c][0]; ow[128 + c * 16] = oc[c][1]; }
    }
    if (bump_flag(FL_OW) == 3) {
      f32x4 t[4];
#pragma unroll
      for (int u = 0; u < 4; ++u) t[u] = *(const LAS f32x4*)(smem + oadr + u * 1024);
      const float l0 = *(const LAS float*)(smem + ladr), l1 = *(const LAS float*)(smem + ladr + 8), l2 = *(const LAS float*)(smem + ladr + 16), l3 = *(const LAS float*)(smem + ladr + 24);
      SB();
      const f32x4 o = (t[0] + t[1]) + (t[2] + t[3]);
      st_quad(rws, ob, quad(o[0], o[1], o[2], o[3], tag));
      st_quad(rws, mlb, quad((lane >> 5) ? m1 : m0, (l0 + l1) + (l2 + l3), 0.f, 0.f, tag));
      set_flag(FL_OPUB);
    }
  } else if (wave < 10) {
    __builtin_amdgcn_s_setprio(1);
    const int w2 = wave & 1;
    const int r = RBLK(j), c4 = j & 3;
    const rsrc_t rw = mk(a.wo, NQ * D * 2);
    const rsrc_t rws = mk(a.ws, WS_END);
    v4u warm[4];
    if (w2 == 0) {
      const int k = j & 31, x = j >> 5, xr = (x + RSHIFT / 8) & 7;
      const unsigned char* p = (const unsigned char*)a.x;
      if (k < 2) p = (const unsigned char*)(a.vc + ((size_t)(2 * x + k) * SK + (SK - WIN)) * DH);
      else if (k < 4) p = (const unsigned char*)(a.kc + ((size_t)(2 * x + k - 2) * SK + (SK - WIN)) * DH);
      else if (k < 7) p = (const unsigned char*)a.wg + (size_t)(k - 4) * (2u << 20) + 1024 * x;
      else if (k < 10) p = (const unsigned char*)a.wu + (size_t)(k - 7) * (2u << 20) + 1024 * x;
      else if (k < 12) p = (const unsigned char*)a.wo + (size_t)(512 * xr + 511 * (k - 10)) * 1152;
      else if (k < 14) p = (const unsigned char*)a.wd + (size_t)(512 * xr + 511 * (k - 12)) * 1152;
      else if (k == 14) p = (const unsigned char*)a.wd + (size_t)(4096 + 64 * xr) * 1152;
      else if (k < 18) p = (const unsigned char*)a.wq + (size_t)(k - 15) * (2u << 20) + 1024 * x;
      else if (k == 18) p = (const unsigned char*)a.out;
      const u32 v = k < 19 ? *(const GAS u32*)p : 0u;
      warm[0] = warm[1] = warm[2] = warm[3] = v4u{v, v, v, v};
    } else {
      constexpr u32 SL = 3328;
      static_assert(256 * SL >= WS_END - WS_Q, "hand-off region");
#pragma unroll
      for (int i = 0; i < 4; ++i) warm[i] = __builtin_amdgcn_raw_buffer_load_b128(rws, (u32)(i * 1024 + lane * 16) < SL ? WS_Q + j * SL + i * 1024 + lane * 16 : OOB, 0, 0);
    }
    u32 vo[9];
#pragma unroll
    for (int i = 0; i < 9; ++i) {
      const u32 id = (w2 + 2 * i) * 64 + lane;
      const u32 row = (id * 3641u) >> 16, c = id - row * 18;
      vo[i] = (64 * r + row) * 1152 + c4 * 288 + c * 16;
      asm volatile("" : "+v"(vo[i]));
    }
    const Tag tag = gettag();
#pragma unroll
    for (int i = 0; i < 4; ++i) asm volatile("" ::"v"(warm[i]));
    wait_flag(FL_G0, 3);
#pragma unroll
    for (int i = 0; i < 9; ++i) dma16(rw, vo[i], 0, L_WO + (w2 + 2 * i) * 1024);
    __builtin_amdgcn_s_setprio(3);
    VMCNT(0);
    add_flag(FL_WOL);
    wait_flag(FL_WOL, 2);
    const u32 lr = lane >> 4, q4 = (lane & 15) >> 2, p4 = lane & 3;
    const u32 badr = L_WO + (8 * lr + q4) * 288 + p4 * 8 + w2 * 32;
    s16x4 bb[5][4];
#pragma unroll
    for (int t = 0; t < 5; ++t)
#pragma unroll
      for (int u = 0; u < 4; ++u)
        bb[t][u] = __builtin_amdgcn_ds_read_tr16_b64_v4i16((LAS s16x4*)(smem + badr + ((u >> 1) * 32 + (u & 1) * 4) * 288 + ((t == 4 && w2) ? 3 : t) * 64));
    const u32 cqA = c4 * 36 + (w2 + 2 * lr) * 4 + q4, cqB = c4 * 36 + 32 + q4;
    u32 offA = p4 == 0 ? WS_FIN + (((cqA >> 1) * 2) * 64 + r) * 32 + (cqA & 1) * 16 : OOB;
    u32 offB = (w2 == 0 && lr == 0 && p4 == 0) ? WS_FIN + (((cqB >> 1) * 2) * 64 + r) * 32 + (cqB & 1) * 16 : OOB;
    u32 aadr = L_OI + lr * 16;
    asm volatile("" : "+v"(offA), "+v"(offB), "+v"(aadr));
#pragma unroll
    for (int t = 0; t < 5; ++t)
#pragma unroll
      for (int u = 0; u < 4; ++u) asm volatile("" : "+v"(bb[t][u]));
    wait_flag_tight(FL_O, 1);
    const s16x8 a0 = *(const LAS s16x8*)(smem + aadr), a1 = *(const LAS s16x8*)(smem + aadr + 64);
    SB();
    f32x4 acc[5];
#pragma unroll
    for (int t = 0; t < 5; ++t)
      acc[t] = __builtin_amdgcn_mfma_f32_16x16x32_bf16(__builtin_bit_cast(bf16x8, a0), __builtin_bit_cast(bf16x8, __builtin_shufflevector(bb[t][0], bb[t][1], 0, 1, 2, 3, 4, 5, 6, 7)), f32x4{0.f, 0.f, 0.f, 0.f}, 0, 0, 0);
    SB();
#pragma unroll
    for (int t = 0; t < 5; ++t)
      acc[t] = __builtin_amdgcn_mfma_f32_16x16x32_bf16(__builtin_bit_cast(bf16x8, a1), __builtin_bit_cast(bf16x8, __builtin_shufflevector(bb[t][2], bb[t][3], 0, 1, 2, 3, 4, 5, 6, 7)), acc[t], 0, 0, 0);
    SB();
    st_quad(rws, offA, quad_of_lanes(sel4(lr, acc[0][0], acc[1][0], acc[2][0], acc[3][0]), tag));
    if (w2 == 0) st_quad(rws, offB, quad_of_lanes(acc[4][0], tag));
    add_flag(FL_FIN);
  } else if (wave == 10) {
    __builtin_amdgcn_s_setprio(1);
    const int r = RBLK(j), c4 = j & 3;
    const rsrc_t rw = mk(a.wd, F * D * 2);
    const rsrc_t rws = mk(a.ws, WS_END);
    u32 vo[21];
#pragma unroll
    for (int i = 0; i < 21; ++i) {
      const u32 id = i * 64 + lane;
      const u32 row = (id * 3641u) >> 16, c = id - row * 18;
      const u32 grow = row >= 64 ? 4096 + 8 * r + (row - 64) : 64 * r + row;
      vo[i] = id < 1296u ? grow * 1152 + c4 * 288 + c * 16 : OOB;
      asm volatile("" : "+v"(vo[i]));
    }
    const Tag tag = gettag();
    wait_flag(FL_G0, 3);
    wait_flag(FL_MX, 4);
#pragma unroll
    for (int i = 0; i < 21; ++i) dma16(rw, vo[i], 0, L_K + i * 1024);
    __builtin_amdgcn_s_setprio(3);
    VMCNT(DH_LEFT);
    *flg(FL_DH) = 1u;
    VMCNT(0);
    set_flag(FL_DL);
    down_gemv(0, j, lane, tag, rws);
  } else {
    __builtin_amdgcn_s_setprio(3);
    const rsrc_t rws = mk(a.ws, WS_END);
    const int h = RBLK(j) >> 1, hf = RBLK(j) & 1;
    const Tag tag = gettag();
    const u32 lr = lane >> 4;
    {
      const u32 base = WS_Q + (j & ~15) * 256 + lane * 16;
      wait_flag(FL_QPUB, 1);
      __builtin_amdgcn_s_sleep(Q_DELAY);
      wait_flag(FL_VH, 1);
      gather<4>(rws, tag, [&](int i) { return base + i * 1024; }, [&](const v4u (&g)[4]) {
        f32x4 v[4];
#pragma unroll
        for (int i = 0; i < 4; ++i) v[i] = gv4(g[i]);
        float qv[4];
#pragma unroll
        for (int k = 0; k < 4; ++k) qv[k] = swap16_add(swap32_add(v[0][k], v[2][k]), swap32_add(v[1][k], v[3][k]));
        *(LAS v2u*)(smem + L_QI + (lr * 64 + (lane & 15) * 4) * 2) = v2u{pk(qv[0], qv[1]), pk(qv[2], qv[3])};
        set_flag(FL_Q);
      });
    }
    {
      const u32 bo = WS_OP + ((h * 16 + lr) * 32 + hf * 16 + (lane & 15)) * 16;
      const u32 bl = WS_ML + (h * 16 + ((lane & 12) | ((lane + lr) & 3))) * 16;
      wait_flag(FL_OPUB, 1);
      __builtin_amdgcn_s_sleep(O_DELAY);
      wait_flag(FL_DH, 1);
      gather<5>(rws, tag, [&](int i) { return i == 0 ? bl : bo + (i - 1) * 2048; }, [&](const v4u (&g)[5]) {
        const float m = gv(g[0].x), l = gv(g[0].y);
        float M = m; M = fmaxf(M, dpp<DPP_XOR1>(M)); M = fmaxf(M, dpp<DPP_XOR2>(M)); M = fmaxf(M, dpp<DPP_HMIRROR>(M)); M = fmaxf(M, dpp<DPP_MIRROR>(M));
        const float wt = __builtin_amdgcn_exp2f(m - M);
        float L = wt * l; L += dpp<DPP_XOR1>(L); L += dpp<DPP_XOR2>(L); L += dpp<DPP_HMIRROR>(L); L += dpp<DPP_MIRROR>(L);
        f32x4 acc = dpp<0x150>(wt) * gv4(g[1]);
        acc += dpp<0x154>(wt) * gv4(g[2]);
        acc += dpp<0x158>(wt) * gv4(g[3]);
        acc += dpp<0x15C>(wt) * gv4(g[4]);
        const float o = swap16_add(swap32_add(acc[0], acc[2]), swap32_add(acc[1], acc[3])) * __builtin_amdgcn_rcpf(L);
        ((LAS bf16_t*)(smem + L_OI))[(lane & 15) * 4 + lr] = (bf16_t)(pk(o, o) & 0xFFFFu);
        set_flag(FL_O);
      });
    }
  }
}

}

extern "C" void kernel_launch(void* const* d_in, const int* in_sizes, int n_in, void* d_out, int out_size, void* d_ws, size_t ws_size,
                              hipStream_t stream) {
  Args a;
  a.x = (const bf16_t*)d_in[0]; a.kc = (const bf16_t*)d_in[1]; a.vc = (const bf16_t*)d_in[2];
  a.wq = (const bf16_t*)d_in[3]; a.wo = (const bf16_t*)d_in[4];
  a.wg = (const bf16_t*)d_in[6]; a.wu = (const bf16_t*)d_in[7]; a.wd = (const bf16_t*)d_in[8];
  a.out = (bf16_t*)d_out; a.ws = (unsigned char*)d_ws;
  (void)hipFuncSetAttribute((const void*)fused_kernel, hipFuncAttributeMaxDynamicSharedMemorySize, L_END);
  fused_kernel<<<256, NTHR, L_END, stream>>>(a);
}
```
